```python
import torch

def _softmax(x, axis=-1):
    return torch.softmax(x, dim=axis)
def _gelu(x):
    return torch.nn.functional.gelu(x, approximate="tanh")
def _silu(x):
    return torch.nn.functional.silu(x)
def _sigmoid(x):
    return torch.sigmoid(x)
def _layernorm(x, axis=-1, eps=1e-5):
    m = x.mean(dim=axis, keepdim=True)
    v = x.var(dim=axis, keepdim=True, unbiased=False)
    return (x - m) / torch.sqrt(v + eps)
def _scatter_add(tgt, idx, src, axis=0):
    return tgt.clone().index_add_(axis, idx, src)
def _segment_sum(src, seg, n):
    out = torch.zeros((n,) + src.shape[1:], dtype=src.dtype)
    return out.index_add_(0, seg, src)
def _pad(x, pw):
    flat = tuple(v for pair in reversed(pw) for v in pair)
    return torch.nn.functional.pad(x, flat)

kernel_name = 'gated_mix__gqa_block_cached__dense_activation_relu__decode'

def setup_inputs():

    _w = (lambda a: (lambda t: t.to(torch.bfloat16) if t.is_floating_point() else t)(torch.as_tensor(a)))
    def _A_setup():
        B, Sq, D = 4, 1, 448
        Hq, Hkv, Sk, Dh = 16, 8, 8192, 256
        assert Hq % Hkv == 0, (Hq, Hkv)
        return {
            "x": _w((lambda *s: torch.randn(*s, dtype=torch.bfloat16) * 0.05)(B, Sq, D)),
            "k_cache": _w((lambda *s: torch.randn(*s, dtype=torch.bfloat16) * 6.4)(B, Hkv, Sk, Dh)),
            "v_cache": _w((lambda *s: torch.randn(*s, dtype=torch.bfloat16) * 1.6)(B, Hkv, Sk, Dh)),
            "wq": _w((lambda *s: torch.randn(*s, dtype=torch.bfloat16) * 6.4)(D, Hq * Dh) * (D ** -0.5)),
            "wo": _w((lambda *s: torch.randn(*s, dtype=torch.bfloat16) * 1.6)(Hq * Dh, D) * ((Hq * Dh) ** -0.5)),
        }
    _A_in = _A_setup()

    def _B_setup():
        B, S, D, Dff = 4, 1, 448, 1792
        return {
            "x": _w((lambda *s: torch.randn(*s, dtype=torch.bfloat16) * 0.05)(B, S, D)),
            "w1": _w((lambda *s: torch.randn(*s, dtype=torch.bfloat16) * 0.05)(D, Dff)),
            "w2": _w((lambda *s: torch.randn(*s, dtype=torch.bfloat16) * 0.05)(Dff, D)),
        }
    _B_in = _B_setup()

    return {
        "A_x": _A_in["x"],
        "A_k_cache": _A_in["k_cache"],
        "A_v_cache": _A_in["v_cache"],
        "A_wq": _A_in["wq"],
        "A_wo": _A_in["wo"],
        "B_x": _B_in["x"],
        "B_w1": _B_in["w1"],
        "B_w2": _B_in["w2"],
        "gate_w": _w((lambda *s: torch.randn(*s, dtype=torch.bfloat16) * 0.05)(448) * 16.0),
    }

def reference(A_x, A_k_cache, A_v_cache, A_wq, A_wo, B_x, B_w1, B_w2, gate_w):
    def _A_ref(x, k_cache, v_cache, wq, wo):
        B, Sq, D = x.shape
        Hq, Dh = 16, 256
        Hkv = k_cache.shape[1]
        rep = Hq // Hkv
        q = (lambda x, *d: x.permute(*d))((x @ wq).reshape(B, Sq, Hq, Dh), 0, 2, 1, 3)
        k = (lambda x, n, axis: torch.repeat_interleave(x, n, dim=axis))(k_cache, rep, axis=1)
        v = (lambda x, n, axis: torch.repeat_interleave(x, n, dim=axis))(v_cache, rep, axis=1)
        scores = torch.einsum("bhqd,bhkd->bhqk", q, k) * (Dh ** -0.5)
        p = _softmax(scores, axis=-1)
        o = torch.einsum("bhqk,bhkd->bhqd", p, v)
        return (lambda x, *d: x.permute(*d))(o, 0, 2, 1, 3).reshape(B, Sq, Hq * Dh) @ wo

    def _B_ref(x, w1, w2):
        h = torch.einsum("bsd,df->bsf", x, w1)
        zero = torch.zeros_like(h)
        h = torch.maximum(h, zero)
        return torch.einsum("bsf,fd->bsd", h, w2)

    x = A_x
    g = _sigmoid((x * gate_w).sum(-1, keepdims=True))
    a = _A_ref(x, A_k_cache, A_v_cache, A_wq, A_wo)
    b = _B_ref(x, B_w1, B_w2)
    return g * a + (1 - g) * b
```

```cpp
#include <hip/hip_runtime.h>
#include <hip/hip_bf16.h>
#include <cstdint>

typedef unsigned short u16;
typedef unsigned u32x4 __attribute__((ext_vector_type(4)));
typedef float f32x4 __attribute__((ext_vector_type(4)));
typedef __bf16 bf16x2 __attribute__((ext_vector_type(2)));

constexpr int B = 4, D = 448, HQ = 16, HKV = 8, SK = 8192, DH = 256, DFF = 1792;
constexpr int NSPLIT = 16;
constexpr int KEYS_PER_SPLIT = SK / NSPLIT;

__device__ __forceinline__ float bf2f(u16 v) { return __uint_as_float(((unsigned)v) << 16); }
__device__ __forceinline__ u16 f2bf(float f) { __bf16 h = (__bf16)f; return __builtin_bit_cast(u16, h); }
__device__ __forceinline__ float rbf(float f) { return bf2f(f2bf(f)); }
__device__ __forceinline__ float dot2(unsigned k, unsigned q, float acc) {
  return __builtin_amdgcn_fdot2_f32_bf16(__builtin_bit_cast(bf16x2, k), __builtin_bit_cast(bf16x2, q), acc, false);
}

__global__ void __launch_bounds__(256) k_proj(const u16* x, const u16* wq, const u16* w1, u16* q, u16* h) {
  __shared__ float xs[B][D];
  __shared__ float red[4][B][128];
  for (int i = threadIdx.x; i < B * D; i += 256) xs[i / D][i % D] = bf2f(x[i]);
  __syncthreads();
  const int blk = blockIdx.x;
  const bool isq = blk < 32;
  const u16* w = isq ? wq : w1;
  const int N = isq ? HQ * DH : DFF;
  const int c0 = (isq ? blk : blk - 32) * 128;
  const int lane = threadIdx.x & 63, wave = threadIdx.x >> 6;
  float a[B][2] = {};
  for (int k = wave * 112; k < wave * 112 + 112; ++k) {
    unsigned wv = *(const unsigned*)(w + (size_t)k * N + c0 + 2 * lane);
    float w0 = __uint_as_float(wv << 16), w1f = __uint_as_float(wv & 0xffff0000u);
#pragma unroll
    for (int b = 0; b < B; ++b) { a[b][0] = fmaf(xs[b][k], w0, a[b][0]); a[b][1] = fmaf(xs[b][k], w1f, a[b][1]); }
  }
#pragma unroll
  for (int b = 0; b < B; ++b) { red[wave][b][2 * lane] = a[b][0]; red[wave][b][2 * lane + 1] = a[b][1]; }
  __syncthreads();
  for (int i = threadIdx.x; i < B * 128; i += 256) {
    int b = i >> 7, c = i & 127;
    float s = red[0][b][c] + red[1][b][c] + red[2][b][c] + red[3][b][c];
    if (isq) q[b * HQ * DH + c0 + c] = f2bf(s);
    else h[b * DFF + c0 + c] = f2bf(fmaxf(rbf(s), 0.f));
  }
}

__global__ void __launch_bounds__(256) k_attn(const u16* q, const u16* kc, const u16* vc, float* pml, float* po) {
  __shared__ float sm[4][2][2];
  __shared__ float sacc[4][2][2][DH];
  const int s = blockIdx.x, kvh = blockIdx.y, b = blockIdx.z;
  const int lane = threadIdx.x & 63, wave = threadIdx.x >> 6;
  const int half = lane >> 5, c = lane & 31;
  u32x4 qv[2];
#pragma unroll
  for (int h = 0; h < 2; ++h) qv[h] = *(const u32x4*)(q + b * HQ * DH + (kvh * 2 + h) * DH + c * 8);
  const size_t base = ((size_t)(b * HKV + kvh) * SK + (size_t)s * KEYS_PER_SPLIT) * DH;
  const u16* kp = kc + base + (size_t)lane * 8;
  const u16* vp = vc + base + (size_t)lane * 8;
  float m[2] = {-INFINITY, -INFINITY}, l[2] = {0.f, 0.f};
  float acc[2][8] = {};
  constexpr int T = 8;
  const float C2 = 0.0625f * 1.4426950408889634f;
  for (int it = wave; it < KEYS_PER_SPLIT / (2 * T); it += 4) {
    u32x4 kr[T], vr[T];
#pragma unroll
    for (int t = 0; t < T; ++t) kr[t] = __builtin_nontemporal_load((const u32x4*)(kp + ((size_t)it * T + t) * 512));
#pragma unroll
    for (int t = 0; t < T; ++t) vr[t] = __builtin_nontemporal_load((const u32x4*)(vp + ((size_t)it * T + t) * 512));
    float sc[T][2];
#pragma unroll
    for (int t = 0; t < T; ++t)
#pragma unroll
      for (int h = 0; h < 2; ++h) {
        float d = 0.f;
#pragma unroll
        for (int j = 0; j < 4; ++j) d = dot2(kr[t][j], qv[h][j], d);
#pragma unroll
        for (int o = 1; o < 32; o <<= 1) d += __shfl_xor(d, o);
        sc[t][h] = d * C2;
      }
#pragma unroll
    for (int h = 0; h < 2; ++h) {
      float mx = m[h];
#pragma unroll
      for (int t = 0; t < T; ++t) mx = fmaxf(mx, sc[t][h]);
      float al = exp2f(m[h] - mx);
      m[h] = mx;
      l[h] *= al;
#pragma unroll
      for (int j = 0; j < 8; ++j) acc[h][j] *= al;
#pragma unroll
      for (int t = 0; t < T; ++t) {
        float p = exp2f(sc[t][h] - mx);
        l[h] += p;
#pragma unroll
        for (int j = 0; j < 4; ++j) {
          acc[h][2 * j] = fmaf(p, __uint_as_float(vr[t][j] << 16), acc[h][2 * j]);
          acc[h][2 * j + 1] = fmaf(p, __uint_as_float(vr[t][j] & 0xffff0000u), acc[h][2 * j + 1]);
        }
      }
    }
  }
  __shared__ float sml[8][2][2];
#pragma unroll
  for (int h = 0; h < 2; ++h) {
    if (c == 0) { sml[wave * 2 + half][h][0] = m[h]; sml[wave * 2 + half][h][1] = l[h]; }
#pragma unroll
    for (int j = 0; j < 8; ++j) sacc[wave][half][h][c * 8 + j] = acc[h][j];
  }
  __syncthreads();
  (void)sm;
  for (int i = threadIdx.x; i < 2 * DH; i += 256) {
    int h = i >> 8, d = i & 255;
    float M = -INFINITY;
    for (int p = 0; p < 8; ++p) M = fmaxf(M, sml[p][h][0]);
    float L = 0.f, o = 0.f;
    for (int p = 0; p < 8; ++p) { float w = exp2f(sml[p][h][0] - M); L += w * sml[p][h][1]; o += w * sacc[p >> 1][p & 1][h][d]; }
    size_t slot = ((size_t)(b * HQ + kvh * 2 + h)) * NSPLIT + s;
    po[slot * DH + d] = o;
    if (d == 0) { pml[slot * 2] = M; pml[slot * 2 + 1] = L; }
  }
}

__global__ void __launch_bounds__(256) k_out(const float* pml, const float* po, const u16* h, const u16* wo, const u16* w2, float* pout) {
  __shared__ float os[B][16];
  const int blk = blockIdx.x;
  const bool isa = blk < 256;
  if (threadIdx.x < 64) {
    int b = threadIdx.x >> 4, r = threadIdx.x & 15;
    if (isa) {
      int row = blk * 16 + r, hq = row >> 8, d = row & 255;
      size_t slot = (size_t)(b * HQ + hq) * NSPLIT;
      float M = -INFINITY;
      for (int s = 0; s < NSPLIT; ++s) M = fmaxf(M, pml[(slot + s) * 2]);
      float L = 0.f, o = 0.f;
      for (int s = 0; s < NSPLIT; ++s) { float w = exp2f(pml[(slot + s) * 2] - M); L += w * pml[(slot + s) * 2 + 1]; o += w * po[(slot + s) * DH + d]; }
      os[b][r] = rbf(o / L);
    } else {
      os[b][r] = bf2f(h[b * DFF + (blk - 256) * 16 + r]);
    }
  }
  __syncthreads();
  const u16* w = isa ? wo + (size_t)blk * 16 * D : w2 + (size_t)(blk - 256) * 16 * D;
  if (threadIdx.x < 224) {
    int c = threadIdx.x * 2;
    float a[B][2] = {};
#pragma unroll
    for (int r = 0; r < 16; ++r) {
      unsigned wv = *(const unsigned*)(w + r * D + c);
      float w0 = __uint_as_float(wv << 16), w1f = __uint_as_float(wv & 0xffff0000u);
#pragma unroll
      for (int b = 0; b < B; ++b) { a[b][0] = fmaf(os[b][r], w0, a[b][0]); a[b][1] = fmaf(os[b][r], w1f, a[b][1]); }
    }
#pragma unroll
    for (int b = 0; b < B; ++b) *(float2*)(pout + ((size_t)blk * B + b) * D + c) = make_float2(a[b][0], a[b][1]);
  }
}

__global__ void __launch_bounds__(256) k_final(const float* pout, const u16* x, const u16* gw, u16* out) {
  __shared__ float ra[4][64], rb[4][64];
  __shared__ float gs;
  const int lane = threadIdx.x & 63, wave = threadIdx.x >> 6;
  const int o = blockIdx.x * 64 + lane;
  const int b = o / D;
  float sa = 0.f, sb = 0.f;
  for (int p = wave; p < 256; p += 4) sa += pout[(size_t)p * B * D + o];
  for (int p = 256 + wave; p < 368; p += 4) sb += pout[(size_t)p * B * D + o];
  ra[wave][lane] = sa; rb[wave][lane] = sb;
  if (wave == 0) {
    float g = 0.f;
    for (int i = lane; i < D; i += 64) g += rbf(bf2f(x[b * D + i]) * bf2f(gw[i]));
    for (int off = 1; off < 64; off <<= 1) g += __shfl_xor(g, off);
    if (lane == 0) gs = rbf(1.f / (1.f + expf(-rbf(g))));
  }
  __syncthreads();
  if (wave == 0) {
    float a = rbf(ra[0][lane] + ra[1][lane] + ra[2][lane] + ra[3][lane]);
    float bb = rbf(rb[0][lane] + rb[1][lane] + rb[2][lane] + rb[3][lane]);
    float g = gs;
    out[o] = f2bf(rbf(g * a) + rbf(rbf(1.f - g) * bb));
  }
}

extern "C" void kernel_launch(void* const* d_in, const int* in_sizes, int n_in, void* d_out, int out_size, void* d_ws, size_t ws_size, hipStream_t stream) {
  const u16* x = (const u16*)d_in[0];
  const u16* kc = (const u16*)d_in[1];
  const u16* vc = (const u16*)d_in[2];
  const u16* wq = (const u16*)d_in[3];
  const u16* wo = (const u16*)d_in[4];
  const u16* w1 = (const u16*)d_in[6];
  const u16* w2 = (const u16*)d_in[7];
  const u16* gw = (const u16*)d_in[8];
  char* ws = (char*)d_ws;
  u16* q = (u16*)ws;
  u16* h = (u16*)(ws + 65536);
  float* pml = (float*)(ws + 131072);
  float* po = (float*)(ws + 262144);
  float* pout = (float*)(ws + 2097152);
  k_proj<<<46, 256, 0, stream>>>(x, wq, w1, q, h);
  k_attn<<<dim3(NSPLIT, HKV, B), 256, 0, stream>>>(q, kc, vc, pml, po);
  k_out<<<368, 256, 0, stream>>>(pml, po, h, wo, w2, pout);
  k_final<<<28, 256, 0, stream>>>(pout, x, gw, (u16*)d_out);
}
```

```cpp
#include <hip/hip_runtime.h>
#include <stdint.h>

typedef unsigned short u16;
typedef unsigned u32;
typedef unsigned long long u64;
typedef u32 u32x2 __attribute__((ext_vector_type(2)));
typedef u32 u32x4 __attribute__((ext_vector_type(4)));
typedef int i32x4 __attribute__((ext_vector_type(4)));
typedef float f32x4 __attribute__((ext_vector_type(4)));
typedef short s16x4 __attribute__((ext_vector_type(4)));
typedef short s16x8 __attribute__((ext_vector_type(8)));
typedef __bf16 bf16x2 __attribute__((ext_vector_type(2)));
typedef __bf16 bf16x8 __attribute__((ext_vector_type(8)));
#define LAS __attribute__((address_space(3)))
#define GAS __attribute__((address_space(1)))

#define INFL 20
#define NSLOT 8

constexpr int DM = 448, NQ = 4096, SK = 8192, DH = 256, DFF = 1792;
constexpr int NS = NSLOT;
constexpr int SLOT = 16384;
constexpr int F = INFL;
constexpr int UNITS = 32;
constexpr int NDEST = 56;

constexpr u32 WS_EPOCH = 0;
constexpr u32 WS_Q = 4096;
constexpr u32 WS_ML = WS_Q + 32 * 128 * 16;
constexpr u32 WS_P = WS_ML + 32 * 2 * 8 * 16;
constexpr u32 WS_F = WS_P + 32 * 8 * 8 * 32 * 16;
constexpr u32 WS_Y = WS_F + 4 * NDEST * 4096;
constexpr u32 WS_END = WS_Y + 4 * NDEST * 4096;
constexpr u32 WS_STAMP = 4u << 20;

constexpr int L_CTL = NS * SLOT;
constexpr int L_QRED = L_CTL + 128;
constexpr int L_QS = L_QRED + 7 * 256;
constexpr int L_HS = L_QS + 1024;
constexpr int L_OS = L_HS + 64;
constexpr int L_END = L_OS + 128;
enum { C_FULL = 0, C_FREE = 1, C_QCNT = 2, C_TAG = 3, C_HRDY = 4, C_ORDY = 5, C_TAGA = 8, C_TAGB = 9 };

constexpr u32 OOB = 0x7ffffff0u;
constexpr int SC1 = 16;
constexpr u32 SPIN_LDS = 1u << 22, SPIN_MEM = 1u << 17;

#define MEMFENCE() asm volatile("" ::: "memory")
#define LGKM0() asm volatile("s_waitcnt lgkmcnt(0)" ::: "memory")

__device__ __forceinline__ float bf_lo(u32 v) { return __uint_as_float(v << 16); }
__device__ __forceinline__ float bf_hi(u32 v) { return __uint_as_float(v & 0xffff0000u); }
__device__ __forceinline__ u32 pk_bf16(float lo, float hi) {
  bf16x2 r; r[0] = (__bf16)lo; r[1] = (__bf16)hi; return __builtin_bit_cast(u32, r);
}
__device__ __forceinline__ float rbf(float f) { return bf_lo((u32)__builtin_bit_cast(u16, (__bf16)f)); }
__device__ __forceinline__ float dot2(u32 a, u32 b, float acc) {
  return __builtin_amdgcn_fdot2_f32_bf16(__builtin_bit_cast(bf16x2, a), __builtin_bit_cast(bf16x2, b), acc, false);
}
template <int CTRL> __device__ __forceinline__ float dpp(float x) {
  return __builtin_bit_cast(float, __builtin_amdgcn_mov_dpp(__builtin_bit_cast(int, x), CTRL, 0xf, 0xf, true));
}
template <int CTRL> __device__ __forceinline__ u32 dppu(u32 x) { return (u32)__builtin_amdgcn_mov_dpp((int)x, CTRL, 0xf, 0xf, true); }
constexpr int QP_X1 = 0xB1, QP_X2 = 0x4E, QP_0022 = 0xA0, QP_1133 = 0xF5, ROW_HM = 0x141, ROR4 = 0x124, ROR8 = 0x128;
__device__ __forceinline__ float swap32_add(float a, float b) {
  auto s = __builtin_amdgcn_permlane32_swap(__float_as_uint(a), __float_as_uint(b), false, false);
  return __uint_as_float(s[0]) + __uint_as_float(s[1]);
}
__device__ __forceinline__ float swap16_add(float a, float b) {
  auto s = __builtin_amdgcn_permlane16_swap(__float_as_uint(a), __float_as_uint(b), false, false);
  return __uint_as_float(s[0]) + __uint_as_float(s[1]);
}
__device__ __forceinline__ float xrow_max(float x) {
  auto s = __builtin_amdgcn_permlane16_swap(__float_as_uint(x), __float_as_uint(x), false, false);
  x = fmaxf(__uint_as_float(s[0]), __uint_as_float(s[1]));
  auto t = __builtin_amdgcn_permlane32_swap(__float_as_uint(x), __float_as_uint(x), false, false);
  return fmaxf(__uint_as_float(t[0]), __uint_as_float(t[1]));
}

__device__ __forceinline__ u32 lds_ld(volatile LAS u32* p) { return __builtin_amdgcn_readfirstlane(*p); }
__device__ __forceinline__ void lds_wait_ge(volatile LAS u32* p, u32 want) {
#pragma unroll 1
  for (u32 sp = 0; sp < SPIN_LDS; ++sp) {
    if (lds_ld(p) >= want) break;
    __builtin_amdgcn_s_sleep(1);
  }
  MEMFENCE();
}

template <int N>
__device__ __forceinline__ void gather(__amdgpu_buffer_rsrc_t ws, const u32 (&off)[N], const bool (&live)[N], u32 tagA, u32 tagB, u32 (&d0)[N], u32 (&d1)[N]) {
  bool ok[N];
#pragma unroll
  for (int i = 0; i < N; ++i) { ok[i] = !live[i]; d0[i] = 0u; d1[i] = 0u; }
#pragma unroll 1
  for (u32 sp = 0; sp < SPIN_MEM; ++sp) {
    u32x4 t[N];
    MEMFENCE();
#pragma unroll
    for (int i = 0; i < N; ++i) t[i] = __builtin_amdgcn_raw_buffer_load_b128(ws, ok[i] ? OOB : off[i], 0, SC1);
    bool all = true;
#pragma unroll
    for (int i = 0; i < N; ++i) {
      const bool g = !ok[i] && t[i][1] == tagA && t[i][3] == tagB;
      d0[i] = g ? t[i][0] : d0[i]; d1[i] = g ? t[i][2] : d1[i];
      ok[i] = ok[i] || g; all = all && ok[i];
    }
    if (__all(all)) break;
    __builtin_amdgcn_s_sleep(2);
  }
  MEMFENCE();
}
__device__ __forceinline__ void put_granule(__amdgpu_buffer_rsrc_t ws, u32 off, u32 d0, u32 d1, u32 tagA, u32 tagB) {
  u32x4 v = {d0, tagA, d1, tagB};
  __builtin_amdgcn_raw_buffer_store_b128(v, ws, off, 0, SC1);
}
__device__ __forceinline__ float inbox_sum(const u32 (&d0)[4], const u32 (&d1)[4]) {
  float y0 = (__uint_as_float(d0[0]) + __uint_as_float(d0[1])) + (__uint_as_float(d0[2]) + __uint_as_float(d0[3]));
  float y1 = (__uint_as_float(d1[0]) + __uint_as_float(d1[1])) + (__uint_as_float(d1[2]) + __uint_as_float(d1[3]));
  float z = swap32_add(y0, y1);
  z = swap16_add(z, z);
  z += dpp<ROR8>(z);
  z += dpp<ROR4>(z);
  return z;
}

#define STAMP(k) do { } while (0)

__global__ void __launch_bounds__(512, 2)
fused_kernel(const u16* __restrict__ x, const u16* __restrict__ kc, const u16* __restrict__ vc, const u16* __restrict__ wq,
             const u16* __restrict__ wo, const u16* __restrict__ w1, const u16* __restrict__ w2, const u16* __restrict__ gw,
             u16* __restrict__ out, char* __restrict__ ws) {
  __shared__ __attribute__((aligned(16))) char lds_raw[L_END];
  LAS char* lds = (LAS char*)lds_raw;
  volatile LAS u32* ctl = (volatile LAS u32*)(lds + L_CTL);
  const int lane = threadIdx.x & 63;
  const int wave = __builtin_amdgcn_readfirstlane(threadIdx.x >> 6);
  const int pair = blockIdx.x >> 3, j = blockIdx.x & 7;
  const int b = pair >> 3, kvh = pair & 7;
  const int sidx = j * 8 + kvh;
  const __amdgpu_buffer_rsrc_t wsr = __builtin_amdgcn_make_buffer_rsrc(ws, 0, WS_END, 0x00020000);

  if (wave == 0) {
    STAMP(0);
    if (lane < 32) ctl[lane] = 0u;
    LGKM0();
    const u64 kb = (u64)kc + ((u64)pair * SK + (u64)j * (UNITS * 32)) * (DH * 2);
    const u64 vb = (u64)vc + ((u64)pair * SK + (u64)j * (UNITS * 32)) * (DH * 2);
    i32x4 kr, vr;
    kr[0] = (int)(u32)kb; kr[1] = (int)(u32)(kb >> 32); kr[2] = UNITS * SLOT; kr[3] = 0x00020000;
    vr[0] = (int)(u32)vb; vr[1] = (int)(u32)(vb >> 32); vr[2] = UNITS * SLOT; vr[3] = 0x00020000;
    const int h = lane >> 5, p = lane & 31;
    u32 vo[4];
#pragma unroll
    for (int i = 0; i < 4; ++i) vo[i] = h * 512 + ((p ^ (((2 * i + h) & 7) << 1)) << 4);
    const u32 lbase = (u32)(size_t)lds;
    __builtin_amdgcn_s_barrier();
    u32 pub = 0, freed = 0;
    int pos = 0;
    for (int s = 0; s < 2 * UNITS; ++s) {
      if (s >= NS && freed < (u32)(s - NS + 1)) {
        freed = lds_ld(&ctl[C_FREE]);
        if (freed < (u32)(s - NS + 1)) {
          asm volatile("s_waitcnt vmcnt(0)" ::: "memory");
          pub = s; ctl[C_FULL] = pub;
          lds_wait_ge(&ctl[C_FREE], (u32)(s - NS + 1));
          freed = s - NS + 1;
        }
      }
      i32x4 r = (s & 1) ? vr : kr;
      const u32 so = (u32)(s >> 1) * SLOT;
      const u32 lo = lbase + pos * SLOT;
#pragma unroll
      for (int q = 0; q < 4; ++q) {
        asm volatile("s_mov_b32 m0, %0\n\ts_nop 0\n\t"
                     "buffer_load_dwordx4 %1, %5, %6 offen nt lds\n\t"
                     "s_waitcnt vmcnt(%7)\n\t"
                     "buffer_load_dwordx4 %2, %5, %6 offen offset:1024 nt lds\n\t"
                     "s_waitcnt vmcnt(%7)\n\t"
                     "buffer_load_dwordx4 %3, %5, %6 offen offset:2048 nt lds\n\t"
                     "s_waitcnt vmcnt(%7)\n\t"
                     "buffer_load_dwordx4 %4, %5, %6 offen offset:3072 nt lds\n\t"
                     "s_waitcnt vmcnt(%7)"
                     :: "s"(lo + q * 4096), "v"(vo[0]), "v"(vo[1]), "v"(vo[2]), "v"(vo[3]), "s"(r), "s"(so + q * 4096), "n"(F - 1) : "memory");
        const int landed = (16 * s + 4 * q + 5 - F) >> 4;
        if (landed > (int)pub) { pub = landed; ctl[C_FULL] = pub; }
      }
      pos = (pos + 1 == NS) ? 0 : pos + 1;
    }
    STAMP(1);
    asm volatile("s_waitcnt vmcnt(16)" ::: "memory");
    ctl[C_FULL] = 2 * UNITS - 1;
    asm volatile("s_waitcnt vmcnt(0)" ::: "memory");
    ctl[C_FULL] = 2 * UNITS;
    STAMP(2);
    return;
  }

  u32x2 epoch;
  {
    const int r = lane >> 3, c = lane & 7;
    const int row0 = 64 * (wave - 1) + 8 * r;
    const u16* wp = wq + (size_t)row0 * NQ + kvh * 512 + j * 64 + c * 8;
    u32x4 w[8];
#pragma unroll
    for (int i = 0; i < 8; ++i) w[i] = __builtin_nontemporal_load((const u32x4*)(wp + (size_t)i * NQ));
    const u32x4 xv = *(const u32x4*)(x + b * DM + row0);
    u32 eoff = WS_EPOCH + b * 256;
    asm volatile("" : "+v"(eoff));
    epoch = __builtin_amdgcn_raw_buffer_load_b64(wsr, eoff, 0, SC1);
    __builtin_amdgcn_sched_barrier(0);
    __builtin_amdgcn_s_barrier();
    __builtin_amdgcn_sched_barrier(0);
    float a[8];
#pragma unroll
    for (int e = 0; e < 8; ++e) a[e] = 0.f;
#pragma unroll
    for (int i = 0; i < 4; ++i)
#pragma unroll
      for (int d = 0; d < 4; ++d) {
        a[2 * d] = dot2(__builtin_amdgcn_perm(w[2 * i + 1][d], w[2 * i][d], 0x05040100u), xv[i], a[2 * d]);
        a[2 * d + 1] = dot2(__builtin_amdgcn_perm(w[2 * i + 1][d], w[2 * i][d], 0x07060302u), xv[i], a[2 * d + 1]);
      }
    float b4[4], c2[2];
#pragma unroll
    for (int e = 0; e < 4; ++e) b4[e] = swap32_add(a[e], a[e + 4]);
#pragma unroll
    for (int e = 0; e < 2; ++e) c2[e] = swap16_add(b4[e], b4[e + 2]);
    const float t0 = c2[0] + dpp<ROR8>(c2[0]), t1 = c2[1] + dpp<ROR8>(c2[1]);
    const float dsum = (lane & 8) ? t1 : t0;
    ((LAS float*)(lds + L_QRED))[(wave - 1) * 64 + 8 * c + r] = dsum;
    LGKM0();
    if (lane == 0) __hip_atomic_fetch_add((LAS u32*)&ctl[C_QCNT], 1u, __ATOMIC_RELAXED, __HIP_MEMORY_SCOPE_WORKGROUP);
  }

  if (wave == 1) {
    u64 z = ((u64)epoch[0] | ((u64)epoch[1] << 32)) + 0x9E3779B97F4A7C15ull;
    const u64 epoch_next = z;
    z = (z ^ (z >> 30)) * 0xBF58476D1CE4E5B9ull; z = (z ^ (z >> 27)) * 0x94D049BB133111EBull; z ^= z >> 31;
    const u32 tagA = __builtin_amdgcn_readfirstlane((u32)z);
    u32 tagB = __builtin_amdgcn_readfirstlane((u32)(z >> 32));
    if (tagB == tagA) tagB = ~tagA;
    if (lane == 0) {
      ctl[C_TAGA] = tagA; ctl[C_TAGB] = tagB;
      ctl[10] = (u32)epoch_next; ctl[11] = (u32)(epoch_next >> 32);
      ctl[C_TAG] = 1u;
    }
    lds_wait_ge(&ctl[C_QCNT], 7u);
    STAMP(3);
    {
      float s = 0.f;
#pragma unroll
      for (int w = 0; w < 7; ++w) s += ((LAS float*)(lds + L_QRED))[w * 64 + lane];
      const u32 d = pk_bf16(s, dpp<QP_X1>(s));
      const u32 d2 = dppu<QP_X2>(d);
      put_granule(wsr, (lane & 3) == 0 ? WS_Q + (pair * 128 + j * 16 + (lane >> 2)) * 16 : OOB, d, d2, tagA, tagB);
      const u32 off[2] = {WS_Q + (pair * 128 + lane) * 16u, WS_Q + (pair * 128 + 64 + lane) * 16u};
      const bool live[2] = {true, true};
      u32 d0[2], d1[2];
      gather<2>(wsr, off, live, tagA, tagB, d0, d1);
#pragma unroll
      for (int i = 0; i < 2; ++i) *(LAS u32x2*)(lds + L_QS + (i * 64 + lane) * 8) = u32x2{d0[i], d1[i]};
      LGKM0();
    }
    STAMP(4);
    const int n = lane & 15, g = lane >> 4;
    bf16x8 qf[8];
#pragma unroll
    for (int ks = 0; ks < 8; ++ks) qf[ks] = *(LAS bf16x8*)(lds + L_QS + (n & 1) * 512 + ks * 64 + g * 16);
    const u32 ka0 = n * 512 + ((g ^ ((n & 7) << 1)) << 4);
    const int vq = n >> 2, vp = n & 3, vrow = 4 * g + vq;
    const u32 va0 = vrow * 512 + (((vp >> 1) ^ ((vrow & 7) << 1)) << 4) + (vp & 1) * 8;
    f32x4 acc[16];
#pragma unroll
    for (int t = 0; t < 16; ++t) acc[t] = f32x4{0.f, 0.f, 0.f, 0.f};
    float mc = -INFINITY, lsum = 0.f;
    constexpr float C = 0.0625f * 1.4426950408889634f;
    int pos = 0;
    u32 full = 0;
    for (int u = 0; u < UNITS; ++u) {
      if (full < (u32)(2 * u + 1)) { lds_wait_ge(&ctl[C_FULL], 2 * u + 1); full = lds_ld(&ctl[C_FULL]); }
      MEMFENCE();
      LAS char* kbase = lds + pos * SLOT;
      pos = (pos + 1 == NS) ? 0 : pos + 1;
      LAS char* vbase = lds + pos * SLOT;
      pos = (pos + 1 == NS) ? 0 : pos + 1;
      f32x4 s0 = {0.f, 0.f, 0.f, 0.f}, s1 = {0.f, 0.f, 0.f, 0.f};
#pragma unroll
      for (int ks = 0; ks < 8; ++ks) {
        const u32 a = (ka0 ^ ((ks & 3) << 6)) + (ks >> 2) * 256;
        const bf16x8 k0 = *(LAS bf16x8*)(kbase + a);
        const bf16x8 k1 = *(LAS bf16x8*)(kbase + a + 8192);
        s0 = __builtin_amdgcn_mfma_f32_16x16x32_bf16(k0, qf[ks], s0, 0, 0, 0);
        s1 = __builtin_amdgcn_mfma_f32_16x16x32_bf16(k1, qf[ks], s1, 0, 0, 0);
      }
      MEMFENCE();
      ctl[C_FREE] = 2 * u + 1;
      float s[8];
      { const u32 p0 = pk_bf16(s0[0], s0[1]), p1 = pk_bf16(s0[2], s0[3]), p2 = pk_bf16(s1[0], s1[1]), p3 = pk_bf16(s1[2], s1[3]);
        s[0] = bf_lo(p0); s[1] = bf_hi(p0); s[2] = bf_lo(p1); s[3] = bf_hi(p1); s[4] = bf_lo(p2); s[5] = bf_hi(p2); s[6] = bf_lo(p3); s[7] = bf_hi(p3); }
      float mx = fmaxf(fmaxf(fmaxf(s[0], s[1]), fmaxf(s[2], s[3])), fmaxf(fmaxf(s[4], s[5]), fmaxf(s[6], s[7])));
      mx = xrow_max(mx) * C;
      if (__any(mx > mc)) {
        const float mn = fmaxf(mc, mx);
        const float al = __builtin_amdgcn_exp2f(mc - mn);
        mc = mn; lsum *= al;
        const float a0 = dpp<QP_0022>(al), a1 = dpp<QP_1133>(al);
#pragma unroll
        for (int t = 0; t < 16; ++t) { acc[t][0] *= a0; acc[t][1] *= a1; }
      }
      float p[8];
#pragma unroll
      for (int i = 0; i < 8; ++i) p[i] = __builtin_amdgcn_exp2f(fmaf(s[i], C, -mc));
      lsum += ((p[0] + p[1]) + (p[2] + p[3])) + ((p[4] + p[5]) + (p[6] + p[7]));
      const u32x4 pw = {pk_bf16(p[0], p[1]), pk_bf16(p[2], p[3]), pk_bf16(p[4], p[5]), pk_bf16(p[6], p[7])};
      const bf16x8 pf = __builtin_bit_cast(bf16x8, pw);
      if (full < (u32)(2 * u + 2)) { lds_wait_ge(&ctl[C_FULL], 2 * u + 2); full = lds_ld(&ctl[C_FULL]); }
      MEMFENCE();
#pragma unroll
      for (int t = 0; t < 16; ++t) {
        const u32 a = (va0 ^ ((t & 7) << 5)) + (t >> 3) * 256;
        const s16x4 lo = __builtin_amdgcn_ds_read_tr16_b64_v4i16((LAS s16x4*)(vbase + a));
        const s16x4 hi = __builtin_amdgcn_ds_read_tr16_b64_v4i16((LAS s16x4*)(vbase + a + 8192));
        const s16x8 vv = __builtin_shufflevector(lo, hi, 0, 1, 2, 3, 4, 5, 6, 7);
        acc[t] = __builtin_amdgcn_mfma_f32_16x16x32_bf16(pf, __builtin_bit_cast(bf16x8, vv), acc[t], 0, 0, 0);
      }
      MEMFENCE();
      ctl[C_FREE] = 2 * u + 2;
    }
    STAMP(5);
    lsum = swap16_add(lsum, lsum);
    lsum = swap32_add(lsum, lsum);
#pragma unroll
    for (int h = 0; h < 2; ++h)
#pragma unroll
      for (int hf = 0; hf < 2; ++hf) {
        float v[2];
#pragma unroll
        for (int e = 0; e < 2; ++e) {
          const int o = 2 * hf + e;
          const float x01 = (g & 1) ? acc[4 + o][h] : acc[o][h];
          const float x23 = (g & 1) ? acc[12 + o][h] : acc[8 + o][h];
          v[e] = (g & 2) ? x23 : x01;
        }
        put_granule(wsr, WS_P + ((((pair * 8 + h * 4 + g) * 8 + j) * 32) + hf * 16 + n) * 16, __float_as_uint(v[0]), __float_as_uint(v[1]), tagA, tagB);
      }
    put_granule(wsr, lane < 2 ? WS_ML + ((pair * 2 + lane) * 8 + j) * 16 : OOB, __float_as_uint(mc), __float_as_uint(lsum), tagA, tagB);
    {
      const u32 pb = WS_P + ((pair * 8 + j) * 256 + lane) * 16u;
      const u32 off[5] = {pb, pb + 1024, pb + 2048, pb + 3072, WS_ML + ((pair * 2 + (j >> 2)) * 8 + (lane & 7)) * 16u};
      const bool live[5] = {true, true, true, true, true};
      u32 d0[5], d1[5];
      gather<5>(wsr, off, live, tagA, tagB, d0, d1);
      STAMP(6);
      const float ms = __uint_as_float(d0[4]), ls = __uint_as_float(d1[4]);
      float M = fmaxf(ms, dpp<QP_X1>(ms)); M = fmaxf(M, dpp<QP_X2>(M)); M = fmaxf(M, dpp<ROW_HM>(M));
      const float w = __builtin_amdgcn_exp2f(ms - M);
      float L = w * ls; L += dpp<QP_X1>(L); L += dpp<QP_X2>(L); L += dpp<ROW_HM>(L);
      float o0 = 0.f, o1 = 0.f;
#pragma unroll
      for (int i = 0; i < 4; ++i) {
        const float we = __uint_as_float(__builtin_amdgcn_readlane(__float_as_uint(w), 2 * i));
        const float wd = __uint_as_float(__builtin_amdgcn_readlane(__float_as_uint(w), 2 * i + 1));
        const float wi = lane < 32 ? we : wd;
        o0 = fmaf(wi, __uint_as_float(d0[i]), o0); o1 = fmaf(wi, __uint_as_float(d1[i]), o1);
      }
      o0 = swap32_add(o0, o0); o1 = swap32_add(o1, o1);
      const float inv = 1.f / L;
      ((LAS u32*)(lds + L_OS))[lane & 31] = pk_bf16(o0 * inv, o1 * inv);
      LGKM0();
      ctl[C_ORDY] = 1u;
    }
    return;
  }

  if (wave <= 5) {
    const int cl = lane < 56 ? lane : 55;
    const int cp = (wave - 2) * 56 + cl;
    const u32 gofs = ((u32)(b * NDEST + (cp >> 2)) * 256 + sidx * 4 + (cp & 3)) * 16;
    u32 wl[32], wh[32];
    {
      const u16* wp = wo + (size_t)(kvh * 512 + j * 64) * DM + cp * 2;
#pragma unroll
      for (int gi = 0; gi < 32; ++gi) {
        const int ra = (gi >> 4) * 32 + (gi & 15);
        const u32 ua = __builtin_nontemporal_load((const u32*)(wp + ra * DM));
        const u32 ub = __builtin_nontemporal_load((const u32*)(wp + (ra + 16) * DM));
        wl[gi] = __builtin_amdgcn_perm(ub, ua, 0x05040100u);
        wh[gi] = __builtin_amdgcn_perm(ub, ua, 0x07060302u);
      }
    }
    lds_wait_ge(&ctl[C_TAG], 1u);
    const u32 tagA = lds_ld(&ctl[C_TAGA]), tagB = lds_ld(&ctl[C_TAGB]);
    if (sidx < NDEST) {
      const u16* wp = w2 + (size_t)(sidx * 32) * DM + cp * 2;
      u32 ua[16], ub[16];
#pragma unroll
      for (int i = 0; i < 16; ++i) {
        ua[i] = __builtin_nontemporal_load((const u32*)(wp + (2 * i) * DM));
        ub[i] = __builtin_nontemporal_load((const u32*)(wp + (2 * i + 1) * DM));
      }
      lds_wait_ge(&ctl[C_HRDY], 1u);
      float y0 = 0.f, y1 = 0.f;
#pragma unroll
      for (int i4 = 0; i4 < 4; ++i4) {
        const u32x4 hv = *(LAS u32x4*)(lds + L_HS + i4 * 16);
#pragma unroll
        for (int e = 0; e < 4; ++e) {
          const int i = i4 * 4 + e;
          y0 = dot2(__builtin_amdgcn_perm(ub[i], ua[i], 0x05040100u), hv[e], y0);
          y1 = dot2(__builtin_amdgcn_perm(ub[i], ua[i], 0x07060302u), hv[e], y1);
        }
      }
      put_granule(wsr, lane < 56 ? WS_F + gofs : OOB, __float_as_uint(y0), __float_as_uint(y1), tagA, tagB);
    }
#pragma unroll
    for (int gi = 0; gi < 32; ++gi) asm volatile("" : "+v"(wl[gi]), "+v"(wh[gi]));
    lds_wait_ge(&ctl[C_ORDY], 1u);
    float y0 = 0.f, y1 = 0.f, y2 = 0.f, y3 = 0.f;
#pragma unroll
    for (int i4 = 0; i4 < 8; ++i4) {
      const u32x4 ov = *(LAS u32x4*)(lds + L_OS + i4 * 16);
      y0 = dot2(wl[4 * i4], ov[0], y0); y1 = dot2(wh[4 * i4], ov[0], y1);
      y2 = dot2(wl[4 * i4 + 1], ov[1], y2); y3 = dot2(wh[4 * i4 + 1], ov[1], y3);
      y0 = dot2(wl[4 * i4 + 2], ov[2], y0); y1 = dot2(wh[4 * i4 + 2], ov[2], y1);
      y2 = dot2(wl[4 * i4 + 3], ov[3], y2); y3 = dot2(wh[4 * i4 + 3], ov[3], y3);
    }
    put_granule(wsr, lane < 56 ? WS_Y + gofs : OOB, __float_as_uint(y0 + y2), __float_as_uint(y1 + y3), tagA, tagB);
    if (wave == 2) STAMP(7);
    return;
  }

  if (sidx >= NDEST) return;

  if (wave == 7) {
    const int r = lane >> 2, c = lane & 3;
    const u16* wp = w1 + (size_t)(28 * r) * DFF + sidx * 32 + c * 8;
    u32x4 w[28];
#pragma unroll
    for (int i = 0; i < 28; ++i) w[i] = __builtin_nontemporal_load((const u32x4*)(wp + (size_t)i * DFF));
    u32x2 xv[7];
#pragma unroll
    for (int i = 0; i < 7; ++i) xv[i] = *(const u32x2*)(x + b * DM + 28 * r + 4 * i);
    float a[8];
#pragma unroll
    for (int e = 0; e < 8; ++e) a[e] = 0.f;
#pragma unroll
    for (int i = 0; i < 14; ++i)
#pragma unroll
      for (int d = 0; d < 4; ++d) {
        a[2 * d] = dot2(__builtin_amdgcn_perm(w[2 * i + 1][d], w[2 * i][d], 0x05040100u), xv[i >> 1][i & 1], a[2 * d]);
        a[2 * d + 1] = dot2(__builtin_amdgcn_perm(w[2 * i + 1][d], w[2 * i][d], 0x07060302u), xv[i >> 1][i & 1], a[2 * d + 1]);
      }
    float b4[4], c2[2];
#pragma unroll
    for (int e = 0; e < 4; ++e) b4[e] = swap32_add(a[e], a[e + 4]);
#pragma unroll
    for (int e = 0; e < 2; ++e) c2[e] = swap16_add(b4[e], b4[e + 2]);
    const float t0 = c2[0] + dpp<ROR8>(c2[0]), t1 = c2[1] + dpp<ROR8>(c2[1]);
    float dsum = (lane & 8) ? t1 : t0;
    dsum += __shfl_xor(dsum, 4);
    const float hv = fmaxf(rbf(dsum), 0.f);
    ((LAS u16*)(lds + L_HS))[8 * c + (lane >> 3)] = (u16)(__float_as_uint(hv) >> 16);
    LGKM0();
    ctl[C_HRDY] = 1u;
    return;
  }

  {
    float gsum = 0.f;
#pragma unroll
    for (int i = 0; i < 7; ++i) gsum += rbf(bf_lo(x[b * DM + i * 64 + lane]) * bf_lo(gw[i * 64 + lane]));
#pragma unroll
    for (int o = 1; o < 64; o <<= 1) gsum += __shfl_xor(gsum, o);
    const float gate = rbf(1.f / (1.f + __expf(-rbf(gsum))));
    lds_wait_ge(&ctl[C_TAG], 1u);
    const u32 tagA = lds_ld(&ctl[C_TAGA]), tagB = lds_ld(&ctl[C_TAGB]);
    const u32 ib = ((u32)(b * NDEST + sidx) * 256 + lane) * 16;
    u32 d0[4], d1[4];
    lds_wait_ge(&ctl[C_FREE], UNITS);
    {
      const u32 off[4] = {WS_F + ib, WS_F + ib + 1024, WS_F + ib + 2048, WS_F + ib + 3072};
      const bool live[4] = {true, true, true, lane < 32};
      gather<4>(wsr, off, live, tagA, tagB, d0, d1);
    }
    const float bb = rbf(rbf(1.f - gate) * rbf(inbox_sum(d0, d1)));
    lds_wait_ge(&ctl[C_ORDY], 1u);
    {
      const u32 off[4] = {WS_Y + ib, WS_Y + ib + 1024, WS_Y + ib + 2048, WS_Y + ib + 3072};
      const bool live[4] = {true, true, true, true};
      gather<4>(wsr, off, live, tagA, tagB, d0, d1);
    }
    STAMP(8);
    const float aa = rbf(gate * rbf(inbox_sum(d0, d1)));
    const float res = aa + bb;
    if ((lane & 28) == 0) out[b * DM + sidx * 8 + 2 * (lane & 3) + (lane >> 5)] = __builtin_bit_cast(u16, (__bf16)res);
    if (sidx == 0 && lane == 0) {
      const u64 en = (u64)ctl[10] | ((u64)ctl[11] << 32);
      __hip_atomic_store((GAS u64*)(ws + WS_EPOCH + b * 256), en, __ATOMIC_RELAXED, __HIP_MEMORY_SCOPE_AGENT);
    }
    STAMP(9);
  }
}

extern "C" void kernel_launch(void* const* d_in, const int* in_sizes, int n_in, void* d_out, int out_size, void* d_ws, size_t ws_size, hipStream_t stream) {
  fused_kernel<<<256, 512, 0, stream>>>((const u16*)d_in[0], (const u16*)d_in[1], (const u16*)d_in[2], (const u16*)d_in[3], (const u16*)d_in[4],
                                        (const u16*)d_in[6], (const u16*)d_in[7], (const u16*)d_in[8], (u16*)d_out, (char*)d_ws);
}
```

```cpp
#include <hip/hip_runtime.h>
#include <stdint.h>

typedef unsigned short u16;
typedef unsigned u32;
typedef unsigned long long u64;
typedef u32 u32x2 __attribute__((ext_vector_type(2)));
typedef u32 u32x4 __attribute__((ext_vector_type(4)));
typedef int i32x4 __attribute__((ext_vector_type(4)));
typedef float f32x4 __attribute__((ext_vector_type(4)));
typedef short s16x4 __attribute__((ext_vector_type(4)));
typedef short s16x8 __attribute__((ext_vector_type(8)));
typedef __bf16 bf16x2 __attribute__((ext_vector_type(2)));
typedef __bf16 bf16x8 __attribute__((ext_vector_type(8)));
#define LAS __attribute__((address_space(3)))
#define GAS __attribute__((address_space(1)))

#define INFL 20
#define KPOL "nt"
#define VPOL ""
#define NSLOT 8

constexpr int DM = 448, NQ = 4096, SK = 8192, DH = 256, DFF = 1792;
constexpr int NS = NSLOT;
constexpr int SLOT = 16384;
constexpr int F = INFL;
constexpr int UNITS = 32;
constexpr int NDEST = 56;

constexpr u32 WS_EPOCH = 0;
constexpr u32 WS_Q = 4096;
constexpr u32 WS_ML = WS_Q + 32 * 128 * 16;
constexpr u32 WS_P = WS_ML + 32 * 2 * 8 * 16;
constexpr u32 WS_F = WS_P + 32 * 8 * 8 * 32 * 16;
constexpr u32 WS_Y = WS_F + 4 * NDEST * 4096;
constexpr u32 WS_END = WS_Y + 4 * NDEST * 4096;
constexpr u32 WS_STAMP = 4u << 20;

constexpr int L_CTL = NS * SLOT;
constexpr int L_QRED = L_CTL + 128;
constexpr int L_QS = L_QRED + 7 * 256;
constexpr int L_HS = L_QS + 1024;
constexpr int L_OS = L_HS + 64;
constexpr int L_END = L_OS + 128;
enum { C_FULL = 0, C_FREE = 1, C_QCNT = 2, C_TAG = 3, C_HRDY = 4, C_ORDY = 5, C_TAGA = 8, C_TAGB = 9 };

constexpr u32 OOB = 0x7ffffff0u;
constexpr int SC1 = 16;
constexpr u32 SPIN_LDS = 1u << 22, SPIN_MEM = 1u << 17;

#define MEMFENCE() asm volatile("" ::: "memory")
#define LGKM0() asm volatile("s_waitcnt lgkmcnt(0)" ::: "memory")

__device__ __forceinline__ float bf_lo(u32 v) { return __uint_as_float(v << 16); }
__device__ __forceinline__ float bf_hi(u32 v) { return __uint_as_float(v & 0xffff0000u); }
__device__ __forceinline__ u32 pk_bf16(float lo, float hi) {
  bf16x2 r; r[0] = (__bf16)lo; r[1] = (__bf16)hi; return __builtin_bit_cast(u32, r);
}
__device__ __forceinline__ float rbf(float f) { return bf_lo((u32)__builtin_bit_cast(u16, (__bf16)f)); }
__device__ __forceinline__ float dot2(u32 a, u32 b, float acc) {
  return __builtin_amdgcn_fdot2_f32_bf16(__builtin_bit_cast(bf16x2, a), __builtin_bit_cast(bf16x2, b), acc, false);
}
template <int CTRL> __device__ __forceinline__ float dpp(float x) {
  return __builtin_bit_cast(float, __builtin_amdgcn_mov_dpp(__builtin_bit_cast(int, x), CTRL, 0xf, 0xf, true));
}
template <int CTRL> __device__ __forceinline__ u32 dppu(u32 x) { return (u32)__builtin_amdgcn_mov_dpp((int)x, CTRL, 0xf, 0xf, true); }
constexpr int QP_X1 = 0xB1, QP_X2 = 0x4E, QP_0022 = 0xA0, QP_1133 = 0xF5, ROW_HM = 0x141, ROR4 = 0x124, ROR8 = 0x128;
__device__ __forceinline__ float swap32_add(float a, float b) {
  auto s = __builtin_amdgcn_permlane32_swap(__float_as_uint(a), __float_as_uint(b), false, false);
  return __uint_as_float(s[0]) + __uint_as_float(s[1]);
}
__device__ __forceinline__ float swap16_add(float a, float b) {
  auto s = __builtin_amdgcn_permlane16_swap(__float_as_uint(a), __float_as_uint(b), false, false);
  return __uint_as_float(s[0]) + __uint_as_float(s[1]);
}
__device__ __forceinline__ float xrow_max(float x) {
  auto s = __builtin_amdgcn_permlane16_swap(__float_as_uint(x), __float_as_uint(x), false, false);
  x = fmaxf(__uint_as_float(s[0]), __uint_as_float(s[1]));
  auto t = __builtin_amdgcn_permlane32_swap(__float_as_uint(x), __float_as_uint(x), false, false);
  return fmaxf(__uint_as_float(t[0]), __uint_as_float(t[1]));
}

__device__ __forceinline__ u32 lds_ld(volatile LAS u32* p) { return __builtin_amdgcn_readfirstlane(*p); }
__device__ __forceinline__ void lds_wait_ge(volatile LAS u32* p, u32 want) {
#pragma unroll 1
  for (u32 sp = 0; sp < SPIN_LDS; ++sp) {
    if (lds_ld(p) >= want) break;
    __builtin_amdgcn_s_sleep(1);
  }
  MEMFENCE();
}

template <int N>
__device__ __forceinline__ void gather(__amdgpu_buffer_rsrc_t ws, const u32 (&off)[N], const bool (&live)[N], u32 tagA, u32 tagB, u32 (&d0)[N], u32 (&d1)[N]) {
  bool ok[N];
#pragma unroll
  for (int i = 0; i < N; ++i) { ok[i] = !live[i]; d0[i] = 0u; d1[i] = 0u; }
#pragma unroll 1
  for (u32 sp = 0; sp < SPIN_MEM; ++sp) {
    u32x4 t[N];
    MEMFENCE();
#pragma unroll
    for (int i = 0; i < N; ++i) t[i] = __builtin_amdgcn_raw_buffer_load_b128(ws, ok[i] ? OOB : off[i], 0, SC1);
    bool all = true;
#pragma unroll
    for (int i = 0; i < N; ++i) {
      const bool g = !ok[i] && t[i][1] == tagA && t[i][3] == tagB;
      d0[i] = g ? t[i][0] : d0[i]; d1[i] = g ? t[i][2] : d1[i];
      ok[i] = ok[i] || g; all = all && ok[i];
    }
    if (__all(all)) break;
    __builtin_amdgcn_s_sleep(2);
  }
  MEMFENCE();
}
__device__ __forceinline__ void put_granule(__amdgpu_buffer_rsrc_t ws, u32 off, u32 d0, u32 d1, u32 tagA, u32 tagB) {
  u32x4 v = {d0, tagA, d1, tagB};
  __builtin_amdgcn_raw_buffer_store_b128(v, ws, off, 0, SC1);
}
__device__ __forceinline__ float inbox_sum(const u32 (&d0)[4], const u32 (&d1)[4]) {
  float y0 = (__uint_as_float(d0[0]) + __uint_as_float(d0[1])) + (__uint_as_float(d0[2]) + __uint_as_float(d0[3]));
  float y1 = (__uint_as_float(d1[0]) + __uint_as_float(d1[1])) + (__uint_as_float(d1[2]) + __uint_as_float(d1[3]));
  float z = swap32_add(y0, y1);
  z = swap16_add(z, z);
  z += dpp<ROR8>(z);
  z += dpp<ROR4>(z);
  return z;
}

#define STAMP(k) do { } while (0)

__global__ void __launch_bounds__(512, 2)
fused_kernel(const u16* __restrict__ x, const u16* __restrict__ kc, const u16* __restrict__ vc, const u16* __restrict__ wq,
             const u16* __restrict__ wo, const u16* __restrict__ w1, const u16* __restrict__ w2, const u16* __restrict__ gw,
             u16* __restrict__ out, char* __restrict__ ws) {
  __shared__ __attribute__((aligned(16))) char lds_raw[L_END];
  LAS char* lds = (LAS char*)lds_raw;
  volatile LAS u32* ctl = (volatile LAS u32*)(lds + L_CTL);
  const int lane = threadIdx.x & 63;
  const int wave = __builtin_amdgcn_readfirstlane(threadIdx.x >> 6);
  const int pair = blockIdx.x >> 3, j = blockIdx.x & 7;
  const int b = pair >> 3, kvh = pair & 7;
  const int sidx = j * 8 + kvh;
  const __amdgpu_buffer_rsrc_t wsr = __builtin_amdgcn_make_buffer_rsrc(ws, 0, WS_END, 0x00020000);

  if (wave == 0) {
    STAMP(0);
    if (lane < 32) ctl[lane] = 0u;
    LGKM0();
    const u64 kb = (u64)kc + ((u64)pair * SK + (u64)j * (UNITS * 32)) * (DH * 2);
    const u64 vb = (u64)vc + ((u64)pair * SK + (u64)j * (UNITS * 32)) * (DH * 2);
    i32x4 kr, vr;
    kr[0] = (int)(u32)kb; kr[1] = (int)(u32)(kb >> 32); kr[2] = UNITS * SLOT; kr[3] = 0x00020000;
    vr[0] = (int)(u32)vb; vr[1] = (int)(u32)(vb >> 32); vr[2] = UNITS * SLOT; vr[3] = 0x00020000;
    const int h = lane >> 5, p = lane & 31;
    u32 vo[4];
#pragma unroll
    for (int i = 0; i < 4; ++i) vo[i] = h * 512 + ((p ^ (((2 * i + h) & 7) << 1)) << 4);
    const u32 lbase = (u32)(size_t)lds;
    __builtin_amdgcn_s_barrier();
    u32 pub = 0, freed = 0;
    int pos = 0;
    for (int s = 0; s < 2 * UNITS; ++s) {
      if (s >= NS && freed < (u32)(s - NS + 1)) {
        freed = lds_ld(&ctl[C_FREE]);
        if (freed < (u32)(s - NS + 1)) {
          asm volatile("s_waitcnt vmcnt(0)" ::: "memory");
          pub = s; ctl[C_FULL] = pub;
          lds_wait_ge(&ctl[C_FREE], (u32)(s - NS + 1));
          freed = s - NS + 1;
        }
      }
      i32x4 r = (s & 1) ? vr : kr;
      const u32 so = (u32)(s >> 1) * SLOT;
      const u32 lo = lbase + pos * SLOT;
#pragma unroll
      for (int q = 0; q < 4; ++q) {
#define DMA4(POL) asm volatile("s_mov_b32 m0, %0\n\ts_nop 0\n\t" \
                     "buffer_load_dwordx4 %1, %5, %6 offen " POL " lds\n\t" \
                     "s_waitcnt vmcnt(%7)\n\t" \
                     "buffer_load_dwordx4 %2, %5, %6 offen offset:1024 " POL " lds\n\t" \
                     "s_waitcnt vmcnt(%7)\n\t" \
                     "buffer_load_dwordx4 %3, %5, %6 offen offset:2048 " POL " lds\n\t" \
                     "s_waitcnt vmcnt(%7)\n\t" \
                     "buffer_load_dwordx4 %4, %5, %6 offen offset:3072 " POL " lds\n\t" \
                     "s_waitcnt vmcnt(%7)" \
                     :: "s"(lo + q * 4096), "v"(vo[0]), "v"(vo[1]), "v"(vo[2]), "v"(vo[3]), "s"(r), "s"(so + q * 4096), "n"(F - 1) : "memory")
        if (s & 1) DMA4(VPOL); else DMA4(KPOL);
        const int landed = (16 * s + 4 * q + 5 - F) >> 4;
        if (landed > (int)pub) { pub = landed; ctl[C_FULL] = pub; }
      }
      pos = (pos + 1 == NS) ? 0 : pos + 1;
    }
    STAMP(1);
    asm volatile("s_waitcnt vmcnt(16)" ::: "memory");
    ctl[C_FULL] = 2 * UNITS - 1;
    asm volatile("s_waitcnt vmcnt(0)" ::: "memory");
    ctl[C_FULL] = 2 * UNITS;
    STAMP(2);
    return;
  }

  u32x2 epoch;
  {
    const int r = lane >> 3, c = lane & 7;
    const int row0 = 64 * (wave - 1) + 8 * r;
    const u16* wp = wq + (size_t)row0 * NQ + kvh * 512 + j * 64 + c * 8;
    u32x4 w[8];
#pragma unroll
    for (int i = 0; i < 8; ++i) w[i] = __builtin_nontemporal_load((const u32x4*)(wp + (size_t)i * NQ));
    const u32x4 xv = *(const u32x4*)(x + b * DM + row0);
    u32 eoff = WS_EPOCH + b * 256;
    asm volatile("" : "+v"(eoff));
    epoch = __builtin_amdgcn_raw_buffer_load_b64(wsr, eoff, 0, SC1);
    __builtin_amdgcn_sched_barrier(0);
    __builtin_amdgcn_s_barrier();
    __builtin_amdgcn_sched_barrier(0);
    float a[8];
#pragma unroll
    for (int e = 0; e < 8; ++e) a[e] = 0.f;
#pragma unroll
    for (int i = 0; i < 4; ++i)
#pragma unroll
      for (int d = 0; d < 4; ++d) {
        a[2 * d] = dot2(__builtin_amdgcn_perm(w[2 * i + 1][d], w[2 * i][d], 0x05040100u), xv[i], a[2 * d]);
        a[2 * d + 1] = dot2(__builtin_amdgcn_perm(w[2 * i + 1][d], w[2 * i][d], 0x07060302u), xv[i], a[2 * d + 1]);
      }
    float b4[4], c2[2];
#pragma unroll
    for (int e = 0; e < 4; ++e) b4[e] = swap32_add(a[e], a[e + 4]);
#pragma unroll
    for (int e = 0; e < 2; ++e) c2[e] = swap16_add(b4[e], b4[e + 2]);
    const float t0 = c2[0] + dpp<ROR8>(c2[0]), t1 = c2[1] + dpp<ROR8>(c2[1]);
    const float dsum = (lane & 8) ? t1 : t0;
    ((LAS float*)(lds + L_QRED))[(wave - 1) * 64 + 8 * c + r] = dsum;
    LGKM0();
    if (lane == 0) __hip_atomic_fetch_add((LAS u32*)&ctl[C_QCNT], 1u, __ATOMIC_RELAXED, __HIP_MEMORY_SCOPE_WORKGROUP);
  }

  if (wave == 1) {
    u64 z = ((u64)epoch[0] | ((u64)epoch[1] << 32)) + 0x9E3779B97F4A7C15ull;
    const u64 epoch_next = z;
    z = (z ^ (z >> 30)) * 0xBF58476D1CE4E5B9ull; z = (z ^ (z >> 27)) * 0x94D049BB133111EBull; z ^= z >> 31;
    const u32 tagA = __builtin_amdgcn_readfirstlane((u32)z);
    u32 tagB = __builtin_amdgcn_readfirstlane((u32)(z >> 32));
    if (tagB == tagA) tagB = ~tagA;
    if (lane == 0) {
      ctl[C_TAGA] = tagA; ctl[C_TAGB] = tagB;
      ctl[10] = (u32)epoch_next; ctl[11] = (u32)(epoch_next >> 32);
      ctl[C_TAG] = 1u;
    }
    lds_wait_ge(&ctl[C_QCNT], 7u);
    STAMP(3);
    {
      float s = 0.f;
#pragma unroll
      for (int w = 0; w < 7; ++w) s += ((LAS float*)(lds + L_QRED))[w * 64 + lane];
      const u32 d = pk_bf16(s, dpp<QP_X1>(s));
      const u32 d2 = dppu<QP_X2>(d);
      put_granule(wsr, (lane & 3) == 0 ? WS_Q + (pair * 128 + j * 16 + (lane >> 2)) * 16 : OOB, d, d2, tagA, tagB);
      const u32 off[2] = {WS_Q + (pair * 128 + lane) * 16u, WS_Q + (pair * 128 + 64 + lane) * 16u};
      const bool live[2] = {true, true};
      u32 d0[2], d1[2];
      gather<2>(wsr, off, live, tagA, tagB, d0, d1);
#pragma unroll
      for (int i = 0; i < 2; ++i) *(LAS u32x2*)(lds + L_QS + (i * 64 + lane) * 8) = u32x2{d0[i], d1[i]};
      LGKM0();
    }
    STAMP(4);
    const int n = lane & 15, g = lane >> 4;
    bf16x8 qf[8];
#pragma unroll
    for (int ks = 0; ks < 8; ++ks) qf[ks] = *(LAS bf16x8*)(lds + L_QS + (n & 1) * 512 + ks * 64 + g * 16);
    const u32 ka0 = n * 512 + ((g ^ ((n & 7) << 1)) << 4);
    const int vq = n >> 2, vp = n & 3, vrow = 4 * g + vq;
    const u32 va0 = vrow * 512 + (((vp >> 1) ^ ((vrow & 7) << 1)) << 4) + (vp & 1) * 8;
    f32x4 acc[16];
#pragma unroll
    for (int t = 0; t < 16; ++t) acc[t] = f32x4{0.f, 0.f, 0.f, 0.f};
    float mc = -INFINITY, lsum = 0.f;
    constexpr float C = 0.0625f * 1.4426950408889634f;
    int pos = 0;
    u32 full = 0;
    for (int u = 0; u < UNITS; ++u) {
      if (full < (u32)(2 * u + 1)) { lds_wait_ge(&ctl[C_FULL], 2 * u + 1); full = lds_ld(&ctl[C_FULL]); }
      MEMFENCE();
      LAS char* kbase = lds + pos * SLOT;
      pos = (pos + 1 == NS) ? 0 : pos + 1;
      LAS char* vbase = lds + pos * SLOT;
      pos = (pos + 1 == NS) ? 0 : pos + 1;
      f32x4 s0 = {0.f, 0.f, 0.f, 0.f}, s1 = {0.f, 0.f, 0.f, 0.f};
#pragma unroll
      for (int ks = 0; ks < 8; ++ks) {
        const u32 a = (ka0 ^ ((ks & 3) << 6)) + (ks >> 2) * 256;
        const bf16x8 k0 = *(LAS bf16x8*)(kbase + a);
        const bf16x8 k1 = *(LAS bf16x8*)(kbase + a + 8192);
        s0 = __builtin_amdgcn_mfma_f32_16x16x32_bf16(k0, qf[ks], s0, 0, 0, 0);
        s1 = __builtin_amdgcn_mfma_f32_16x16x32_bf16(k1, qf[ks], s1, 0, 0, 0);
      }
      MEMFENCE();
      ctl[C_FREE] = 2 * u + 1;
      float s[8];
      { const u32 p0 = pk_bf16(s0[0], s0[1]), p1 = pk_bf16(s0[2], s0[3]), p2 = pk_bf16(s1[0], s1[1]), p3 = pk_bf16(s1[2], s1[3]);
        s[0] = bf_lo(p0); s[1] = bf_hi(p0); s[2] = bf_lo(p1); s[3] = bf_hi(p1); s[4] = bf_lo(p2); s[5] = bf_hi(p2); s[6] = bf_lo(p3); s[7] = bf_hi(p3); }
      float mx = fmaxf(fmaxf(fmaxf(s[0], s[1]), fmaxf(s[2], s[3])), fmaxf(fmaxf(s[4], s[5]), fmaxf(s[6], s[7])));
      mx = xrow_max(mx) * C;
      if (__any(mx > mc)) {
        const float mn = fmaxf(mc, mx);
        const float al = __builtin_amdgcn_exp2f(mc - mn);
        mc = mn; lsum *= al;
        const float a0 = dpp<QP_0022>(al), a1 = dpp<QP_1133>(al);
#pragma unroll
        for (int t = 0; t < 16; ++t) { acc[t][0] *= a0; acc[t][1] *= a1; }
      }
      float p[8];
#pragma unroll
      for (int i = 0; i < 8; ++i) p[i] = __builtin_amdgcn_exp2f(fmaf(s[i], C, -mc));
      lsum += ((p[0] + p[1]) + (p[2] + p[3])) + ((p[4] + p[5]) + (p[6] + p[7]));
      const u32x4 pw = {pk_bf16(p[0], p[1]), pk_bf16(p[2], p[3]), pk_bf16(p[4], p[5]), pk_bf16(p[6], p[7])};
      const bf16x8 pf = __builtin_bit_cast(bf16x8, pw);
      if (full < (u32)(2 * u + 2)) { lds_wait_ge(&ctl[C_FULL], 2 * u + 2); full = lds_ld(&ctl[C_FULL]); }
      MEMFENCE();
#pragma unroll
      for (int t = 0; t < 16; ++t) {
        const u32 a = (va0 ^ ((t & 7) << 5)) + (t >> 3) * 256;
        const s16x4 lo = __builtin_amdgcn_ds_read_tr16_b64_v4i16((LAS s16x4*)(vbase + a));
        const s16x4 hi = __builtin_amdgcn_ds_read_tr16_b64_v4i16((LAS s16x4*)(vbase + a + 8192));
        const s16x8 vv = __builtin_shufflevector(lo, hi, 0, 1, 2, 3, 4, 5, 6, 7);
        acc[t] = __builtin_amdgcn_mfma_f32_16x16x32_bf16(pf, __builtin_bit_cast(bf16x8, vv), acc[t], 0, 0, 0);
      }
      MEMFENCE();
      ctl[C_FREE] = 2 * u + 2;
    }
    STAMP(5);
    lsum = swap16_add(lsum, lsum);
    lsum = swap32_add(lsum, lsum);
#pragma unroll
    for (int h = 0; h < 2; ++h)
#pragma unroll
      for (int hf = 0; hf < 2; ++hf) {
        float v[2];
#pragma unroll
        for (int e = 0; e < 2; ++e) {
          const int o = 2 * hf + e;
          const float x01 = (g & 1) ? acc[4 + o][h] : acc[o][h];
          const float x23 = (g & 1) ? acc[12 + o][h] : acc[8 + o][h];
          v[e] = (g & 2) ? x23 : x01;
        }
        put_granule(wsr, WS_P + ((((pair * 8 + h * 4 + g) * 8 + j) * 32) + hf * 16 + n) * 16, __float_as_uint(v[0]), __float_as_uint(v[1]), tagA, tagB);
      }
    put_granule(wsr, lane < 2 ? WS_ML + ((pair * 2 + lane) * 8 + j) * 16 : OOB, __float_as_uint(mc), __float_as_uint(lsum), tagA, tagB);
    {
      const u32 pb = WS_P + ((pair * 8 + j) * 256 + lane) * 16u;
      const u32 off[5] = {pb, pb + 1024, pb + 2048, pb + 3072, WS_ML + ((pair * 2 + (j >> 2)) * 8 + (lane & 7)) * 16u};
      const bool live[5] = {true, true, true, true, true};
      u32 d0[5], d1[5];
      gather<5>(wsr, off, live, tagA, tagB, d0, d1);
      STAMP(6);
      const float ms = __uint_as_float(d0[4]), ls = __uint_as_float(d1[4]);
      float M = fmaxf(ms, dpp<QP_X1>(ms)); M = fmaxf(M, dpp<QP_X2>(M)); M = fmaxf(M, dpp<ROW_HM>(M));
      const float w = __builtin_amdgcn_exp2f(ms - M);
      float L = w * ls; L += dpp<QP_X1>(L); L += dpp<QP_X2>(L); L += dpp<ROW_HM>(L);
      float o0 = 0.f, o1 = 0.f;
#pragma unroll
      for (int i = 0; i < 4; ++i) {
        const float we = __uint_as_float(__builtin_amdgcn_readlane(__float_as_uint(w), 2 * i));
        const float wd = __uint_as_float(__builtin_amdgcn_readlane(__float_as_uint(w), 2 * i + 1));
        const float wi = lane < 32 ? we : wd;
        o0 = fmaf(wi, __uint_as_float(d0[i]), o0); o1 = fmaf(wi, __uint_as_float(d1[i]), o1);
      }
      o0 = swap32_add(o0, o0); o1 = swap32_add(o1, o1);
      const float inv = 1.f / L;
      ((LAS u32*)(lds + L_OS))[lane & 31] = pk_bf16(o0 * inv, o1 * inv);
      LGKM0();
      ctl[C_ORDY] = 1u;
    }
    return;
  }

  if (wave <= 5) {
    const int cl = lane < 56 ? lane : 55;
    const int cp = (wave - 2) * 56 + cl;
    const u32 gofs = ((u32)(b * NDEST + (cp >> 2)) * 256 + sidx * 4 + (cp & 3)) * 16;
    u32 wl[32], wh[32];
    {
      const u16* wp = wo + (size_t)(kvh * 512 + j * 64) * DM + cp * 2;
#pragma unroll
      for (int gi = 0; gi < 32; ++gi) {
        const int ra = (gi >> 4) * 32 + (gi & 15);
        const u32 ua = __builtin_nontemporal_load((const u32*)(wp + ra * DM));
        const u32 ub = __builtin_nontemporal_load((const u32*)(wp + (ra + 16) * DM));
        wl[gi] = __builtin_amdgcn_perm(ub, ua, 0x05040100u);
        wh[gi] = __builtin_amdgcn_perm(ub, ua, 0x07060302u);
      }
    }
    lds_wait_ge(&ctl[C_TAG], 1u);
    const u32 tagA = lds_ld(&ctl[C_TAGA]), tagB = lds_ld(&ctl[C_TAGB]);
    if (sidx < NDEST) {
      const u16* wp = w2 + (size_t)(sidx * 32) * DM + cp * 2;
      u32 ua[16], ub[16];
#pragma unroll
      for (int i = 0; i < 16; ++i) {
        ua[i] = __builtin_nontemporal_load((const u32*)(wp + (2 * i) * DM));
        ub[i] = __builtin_nontemporal_load((const u32*)(wp + (2 * i + 1) * DM));
      }
      lds_wait_ge(&ctl[C_HRDY], 1u);
      float y0 = 0.f, y1 = 0.f;
#pragma unroll
      for (int i4 = 0; i4 < 4; ++i4) {
        const u32x4 hv = *(LAS u32x4*)(lds + L_HS + i4 * 16);
#pragma unroll
        for (int e = 0; e < 4; ++e) {
          const int i = i4 * 4 + e;
          y0 = dot2(__builtin_amdgcn_perm(ub[i], ua[i], 0x05040100u), hv[e], y0);
          y1 = dot2(__builtin_amdgcn_perm(ub[i], ua[i], 0x07060302u), hv[e], y1);
        }
      }
      put_granule(wsr, lane < 56 ? WS_F + gofs : OOB, __float_as_uint(y0), __float_as_uint(y1), tagA, tagB);
    }
#pragma unroll
    for (int gi = 0; gi < 32; ++gi) asm volatile("" : "+v"(wl[gi]), "+v"(wh[gi]));
    lds_wait_ge(&ctl[C_ORDY], 1u);
    float y0 = 0.f, y1 = 0.f, y2 = 0.f, y3 = 0.f;
#pragma unroll
    for (int i4 = 0; i4 < 8; ++i4) {
      const u32x4 ov = *(LAS u32x4*)(lds + L_OS + i4 * 16);
      y0 = dot2(wl[4 * i4], ov[0], y0); y1 = dot2(wh[4 * i4], ov[0], y1);
      y2 = dot2(wl[4 * i4 + 1], ov[1], y2); y3 = dot2(wh[4 * i4 + 1], ov[1], y3);
      y0 = dot2(wl[4 * i4 + 2], ov[2], y0); y1 = dot2(wh[4 * i4 + 2], ov[2], y1);
      y2 = dot2(wl[4 * i4 + 3], ov[3], y2); y3 = dot2(wh[4 * i4 + 3], ov[3], y3);
    }
    put_granule(wsr, lane < 56 ? WS_Y + gofs : OOB, __float_as_uint(y0 + y2), __float_as_uint(y1 + y3), tagA, tagB);
    if (wave == 2) STAMP(7);
    return;
  }

  if (sidx >= NDEST) return;

  if (wave == 7) {
    const int r = lane >> 2, c = lane & 3;
    const u16* wp = w1 + (size_t)(28 * r) * DFF + sidx * 32 + c * 8;
    u32x4 w[28];
#pragma unroll
    for (int i = 0; i < 28; ++i) w[i] = __builtin_nontemporal_load((const u32x4*)(wp + (size_t)i * DFF));
    u32x2 xv[7];
#pragma unroll
    for (int i = 0; i < 7; ++i) xv[i] = *(const u32x2*)(x + b * DM + 28 * r + 4 * i);
    float a[8];
#pragma unroll
    for (int e = 0; e < 8; ++e) a[e] = 0.f;
#pragma unroll
    for (int i = 0; i < 14; ++i)
#pragma unroll
      for (int d = 0; d < 4; ++d) {
        a[2 * d] = dot2(__builtin_amdgcn_perm(w[2 * i + 1][d], w[2 * i][d], 0x05040100u), xv[i >> 1][i & 1], a[2 * d]);
        a[2 * d + 1] = dot2(__builtin_amdgcn_perm(w[2 * i + 1][d], w[2 * i][d], 0x07060302u), xv[i >> 1][i & 1], a[2 * d + 1]);
      }
    float b4[4], c2[2];
#pragma unroll
    for (int e = 0; e < 4; ++e) b4[e] = swap32_add(a[e], a[e + 4]);
#pragma unroll
    for (int e = 0; e < 2; ++e) c2[e] = swap16_add(b4[e], b4[e + 2]);
    const float t0 = c2[0] + dpp<ROR8>(c2[0]), t1 = c2[1] + dpp<ROR8>(c2[1]);
    float dsum = (lane & 8) ? t1 : t0;
    dsum += __shfl_xor(dsum, 4);
    const float hv = fmaxf(rbf(dsum), 0.f);
    ((LAS u16*)(lds + L_HS))[8 * c + (lane >> 3)] = (u16)(__float_as_uint(hv) >> 16);
    LGKM0();
    ctl[C_HRDY] = 1u;
    return;
  }

  {
    float gsum = 0.f;
#pragma unroll
    for (int i = 0; i < 7; ++i) gsum += rbf(bf_lo(x[b * DM + i * 64 + lane]) * bf_lo(gw[i * 64 + lane]));
#pragma unroll
    for (int o = 1; o < 64; o <<= 1) gsum += __shfl_xor(gsum, o);
    const float gate = rbf(1.f / (1.f + __expf(-rbf(gsum))));
    lds_wait_ge(&ctl[C_TAG], 1u);
    const u32 tagA = lds_ld(&ctl[C_TAGA]), tagB = lds_ld(&ctl[C_TAGB]);
    const u32 ib = ((u32)(b * NDEST + sidx) * 256 + lane) * 16;
    u32 d0[4], d1[4];
    lds_wait_ge(&ctl[C_FREE], UNITS);
    {
      const u32 off[4] = {WS_F + ib, WS_F + ib + 1024, WS_F + ib + 2048, WS_F + ib + 3072};
      const bool live[4] = {true, true, true, lane < 32};
      gather<4>(wsr, off, live, tagA, tagB, d0, d1);
    }
    const float bb = rbf(rbf(1.f - gate) * rbf(inbox_sum(d0, d1)));
    lds_wait_ge(&ctl[C_ORDY], 1u);
    {
      const u32 off[4] = {WS_Y + ib, WS_Y + ib + 1024, WS_Y + ib + 2048, WS_Y + ib + 3072};
      const bool live[4] = {true, true, true, true};
      gather<4>(wsr, off, live, tagA, tagB, d0, d1);
    }
    STAMP(8);
    const float aa = rbf(gate * rbf(inbox_sum(d0, d1)));
    const float res = aa + bb;
    if ((lane & 28) == 0) out[b * DM + sidx * 8 + 2 * (lane & 3) + (lane >> 5)] = __builtin_bit_cast(u16, (__bf16)res);
    if (sidx == 0 && lane == 0) {
      const u64 en = (u64)ctl[10] | ((u64)ctl[11] << 32);
      __hip_atomic_store((GAS u64*)(ws + WS_EPOCH + b * 256), en, __ATOMIC_RELAXED, __HIP_MEMORY_SCOPE_AGENT);
    }
    STAMP(9);
  }
}

extern "C" void kernel_launch(void* const* d_in, const int* in_sizes, int n_in, void* d_out, int out_size, void* d_ws, size_t ws_size, hipStream_t stream) {
  fused_kernel<<<256, 512, 0, stream>>>((const u16*)d_in[0], (const u16*)d_in[1], (const u16*)d_in[2], (const u16*)d_in[3], (const u16*)d_in[4],
                                        (const u16*)d_in[6], (const u16*)d_in[7], (const u16*)d_in[8], (u16*)d_out, (char*)d_ws);
}
```

```cpp
#include <hip/hip_runtime.h>
#include <stdint.h>

typedef unsigned short u16;
typedef unsigned u32;
typedef unsigned long long u64;
typedef u32 u32x2 __attribute__((ext_vector_type(2)));
typedef u32 u32x4 __attribute__((ext_vector_type(4)));
typedef int i32x4 __attribute__((ext_vector_type(4)));
typedef float f32x2 __attribute__((ext_vector_type(2)));
typedef float f32x4 __attribute__((ext_vector_type(4)));
typedef short s16x4 __attribute__((ext_vector_type(4)));
typedef short s16x8 __attribute__((ext_vector_type(8)));
typedef __bf16 bf16x2 __attribute__((ext_vector_type(2)));
typedef __bf16 bf16x8 __attribute__((ext_vector_type(8)));
#define LAS __attribute__((address_space(3)))
#define GAS __attribute__((address_space(1)))

#define INFL 24
#define NSLOT 9
#define CKB 12
#define CKG 19
#define SHR 0.5f
#define BAL 1
#define WSLEEP 12
#define FIRSTS 1

constexpr int DM = 448, NQ = 4096, SK = 8192, DH = 256, DFF = 1792;
constexpr int NS = NSLOT;
constexpr int SLOT = 16384;
constexpr int F = INFL;
constexpr int UNITS = 32;
constexpr int SV = 24;
constexpr int NPOOL = 8 * (UNITS - SV);
constexpr int NMAX = 20;
constexpr u32 FINAL = 0x80000000u;
constexpr int NFFN = 56;
constexpr int NDEST = 28;

constexpr u32 WS_EPOCH = 0;
constexpr u32 WS_Q = 4096;
constexpr u32 WS_ML = WS_Q + 32 * 128 * 16;
constexpr u32 WS_P = WS_ML + 32 * 8 * 4 * 16;
constexpr u32 WS_Y = WS_P + 32 * 8 * 8 * 32 * 16;
constexpr u32 WS_S = WS_Y + 4 * NDEST * 8192;
constexpr u32 WS_END = WS_S + 32 * 64 * 128;
constexpr u32 WS_STAMP = 4u << 20;

constexpr int L_CTL = NS * SLOT;
constexpr int L_QRED = L_CTL + 256;
constexpr int L_PA = L_QRED, L_PY = L_QRED + 1024;
constexpr int L_QS = L_QRED + 7 * 256;
constexpr int L_OS = L_QS;
constexpr int L_HS = L_QS + 1024;
constexpr int L_SB = L_HS + 64;
constexpr int L_END = L_SB + UNITS * 128;
static_assert(L_END <= 163840 && L_SB % 16 == 0, "LDS");
enum { C_FREE = 0  , C_FULL = 4, C_QCNT = 5, C_QRDY = 6, C_HRDY = 7, C_OCNT = 8, C_PRDY = 9, C_CRDY = 10, C_YCNT = 11,
       C_GRDY = 12, C_POOL = 13,
       C_TAGA = 16, C_TAGB = 17, C_EP0 = 18, C_EP1 = 19, C_SC0 = 20, C_SC1 = 21, C_IL0 = 22, C_IL1 = 23, C_GATE = 24 };

constexpr u32 OOB = 0x7ffffff0u;
constexpr int SC1 = 16;
constexpr u32 SPIN_LDS = 1u << 22, SPIN_MEM = 1u << 18;

#define MEMFENCE() asm volatile("" ::: "memory")
#define LGKM0() asm volatile("s_waitcnt lgkmcnt(0)" ::: "memory")
#define SCHED() __builtin_amdgcn_sched_barrier(0)
#define PACE() do { SCHED(); MEMFENCE(); if (WSLEEP) __builtin_amdgcn_s_sleep(WSLEEP); MEMFENCE(); SCHED(); } while (0)

__device__ __forceinline__ float bf_lo(u32 v) { return __uint_as_float(v << 16); }
__device__ __forceinline__ float bf_hi(u32 v) { return __uint_as_float(v & 0xffff0000u); }
__device__ __forceinline__ u32 pk_bf16(float lo, float hi) {
  bf16x2 r; r[0] = (__bf16)lo; r[1] = (__bf16)hi; return __builtin_bit_cast(u32, r);
}
__device__ __forceinline__ float rbf(float f) { return bf_lo((u32)__builtin_bit_cast(u16, (__bf16)f)); }
__device__ __forceinline__ float dot2(u32 a, u32 b, float acc) {
  return __builtin_amdgcn_fdot2_f32_bf16(__builtin_bit_cast(bf16x2, a), __builtin_bit_cast(bf16x2, b), acc, false);
}
__device__ __forceinline__ u32 pair_lo(u32 a, u32 b) { return __builtin_amdgcn_perm(b, a, 0x05040100u); }
__device__ __forceinline__ u32 pair_hi(u32 a, u32 b) { return __builtin_amdgcn_perm(b, a, 0x07060302u); }
template <int CTRL> __device__ __forceinline__ float dpp(float x) {
  return __builtin_bit_cast(float, __builtin_amdgcn_mov_dpp(__builtin_bit_cast(int, x), CTRL, 0xf, 0xf, true));
}
template <int CTRL> __device__ __forceinline__ u32 dppu(u32 x) { return (u32)__builtin_amdgcn_mov_dpp((int)x, CTRL, 0xf, 0xf, true); }
constexpr int QP_X1 = 0xB1, QP_X2 = 0x4E, ROR4 = 0x124, ROR8 = 0x128;
__device__ __forceinline__ float swap32_add(float a, float b) {
  auto s = __builtin_amdgcn_permlane32_swap(__float_as_uint(a), __float_as_uint(b), false, false);
  return __uint_as_float(s[0]) + __uint_as_float(s[1]);
}
__device__ __forceinline__ float swap16_add(float a, float b) {
  auto s = __builtin_amdgcn_permlane16_swap(__float_as_uint(a), __float_as_uint(b), false, false);
  return __uint_as_float(s[0]) + __uint_as_float(s[1]);
}
__device__ __forceinline__ float xrow_max(float x) {
  auto s = __builtin_amdgcn_permlane16_swap(__float_as_uint(x), __float_as_uint(x), false, false);
  x = fmaxf(__uint_as_float(s[0]), __uint_as_float(s[1]));
  auto t = __builtin_amdgcn_permlane32_swap(__float_as_uint(x), __float_as_uint(x), false, false);
  return fmaxf(__uint_as_float(t[0]), __uint_as_float(t[1]));
}
__device__ __forceinline__ float reduce8(const float (&a)[8], int lane) {
  float b4[4], c2[2];
#pragma unroll
  for (int e = 0; e < 4; ++e) b4[e] = swap32_add(a[e], a[e + 4]);
#pragma unroll
  for (int e = 0; e < 2; ++e) c2[e] = swap16_add(b4[e], b4[e + 2]);
  const float t0 = c2[0] + dpp<ROR8>(c2[0]), t1 = c2[1] + dpp<ROR8>(c2[1]);
  return (lane & 8) ? t1 : t0;
}

__device__ __forceinline__ u32 lds_ld(volatile LAS u32* p) { return __builtin_amdgcn_readfirstlane(*p); }
template <int SLEEP>
__device__ __forceinline__ u32 lds_wait(volatile LAS u32* p, u32 want) {
  u32 v = 0;
#pragma unroll 1
  for (u32 sp = 0; sp < SPIN_LDS; ++sp) {
    v = lds_ld(p);
    if (v >= want) break;
    if (SLEEP) __builtin_amdgcn_s_sleep(SLEEP);
  }
  MEMFENCE();
  return v;
}
__device__ __forceinline__ u32 free_min(volatile LAS u32* ctl) {
  const u32x4 v = *(volatile LAS u32x4*)(ctl + C_FREE);
  return __builtin_amdgcn_readfirstlane(min(min(v[0], v[1]), min(v[2], v[3])));
}
__device__ __forceinline__ u32 lds_inc(volatile LAS u32* p, int lane) {
  u32 old = 0;
  if (lane == 0) old = __hip_atomic_fetch_add((LAS u32*)p, 1u, __ATOMIC_RELAXED, __HIP_MEMORY_SCOPE_WORKGROUP);
  old = __builtin_amdgcn_readfirstlane(old);
  MEMFENCE();
  return old;
}

template <int N, int SLEEP>
__device__ __forceinline__ void gather(__amdgpu_buffer_rsrc_t ws, const u32 (&off)[N], u32 tagA, u32 tagB, u32 (&d0)[N], u32 (&d1)[N]) {
  u32 o[N];
#pragma unroll
  for (int i = 0; i < N; ++i) { o[i] = off[i]; d0[i] = 0u; d1[i] = 0u; }
#pragma unroll 1
  for (u32 sp = 0; sp < SPIN_MEM; ++sp) {
    u32x4 t[N];
    MEMFENCE();
#pragma unroll
    for (int i = 0; i < N; ++i) t[i] = __builtin_amdgcn_raw_buffer_load_b128(ws, o[i], 0, SC1);
    bool all = true;
#pragma unroll
    for (int i = 0; i < N; ++i) {
      const bool g = t[i][1] == tagA && t[i][3] == tagB;
      d0[i] = g ? t[i][0] : d0[i]; d1[i] = g ? t[i][2] : d1[i];
      o[i] = g ? OOB : o[i];
      all = all && o[i] >= OOB;
    }
    if (__all(all)) break;
    if (SLEEP) __builtin_amdgcn_s_sleep(SLEEP);
  }
  MEMFENCE();
}
__device__ __forceinline__ void put_granule(__amdgpu_buffer_rsrc_t ws, u32 off, u32 d0, u32 d1, u32 tagA, u32 tagB) {
  u32x4 v = {d0, tagA, d1, tagB};
  __builtin_amdgcn_raw_buffer_store_b128(v, ws, off, 0, SC1);
}
__device__ __forceinline__ float inbox_sum(float y0, float y1) {
  float z = swap32_add(y0, y1);
  z = swap16_add(z, z);
  z += dpp<ROR8>(z);
  return z;
}

#define STAMP(k) do { } while (0)

__global__ void __launch_bounds__(512, 2)
fused_kernel(const u16* __restrict__ x, const u16* __restrict__ kc, const u16* __restrict__ vc, const u16* __restrict__ wq,
             const u16* __restrict__ wo, const u16* __restrict__ w1, const u16* __restrict__ w2, const u16* __restrict__ gw,
             u16* __restrict__ out, char* ws) {
  __shared__ __attribute__((aligned(16))) char lds_raw[L_END];
  LAS char* lds = (LAS char*)lds_raw;
  volatile LAS u32* ctl = (volatile LAS u32*)(lds + L_CTL);
  const int lane = threadIdx.x & 63;
  const int wave = __builtin_amdgcn_readfirstlane(threadIdx.x >> 6);
  const int pair = blockIdx.x >> 3, j = blockIdx.x & 7;
  const int b = pair >> 3, kvh = pair & 7;
  const int sidx = j * 8 + kvh;
  const int didx = kvh * 8 + j;
  const __amdgpu_buffer_rsrc_t wsr = __builtin_amdgcn_make_buffer_rsrc(ws, 0, WS_END, 0x00020000);
  const u32 yin = ((u32)(b * NDEST + didx) * 512 + wave * 64 + lane) * 16;

#define DMA_GROUP(LO, R, SO, WAIT) asm volatile("s_mov_b32 m0, %0\n\ts_nop 0\n\t" \
                 "buffer_load_dwordx4 %1, %5, %6 offen nt lds\n\t" \
                 "s_waitcnt vmcnt(%7)\n\t" \
                 "buffer_load_dwordx4 %2, %5, %6 offen offset:1024 nt lds\n\t" \
                 "s_waitcnt vmcnt(%7)\n\t" \
                 "buffer_load_dwordx4 %3, %5, %6 offen offset:2048 nt lds\n\t" \
                 "s_waitcnt vmcnt(%7)\n\t" \
                 "buffer_load_dwordx4 %4, %5, %6 offen offset:3072 nt lds\n\t" \
                 "s_waitcnt vmcnt(%7)" \
                 :: "s"(LO), "v"(vo[0]), "v"(vo[1]), "v"(vo[2]), "v"(vo[3]), "s"(R), "s"(SO), "n"(WAIT) : "memory")
  ctl[lane] = 0u;
  const u64 kb = (u64)kc + (u64)pair * SK * (DH * 2);
  i32x4 kr;
  kr[0] = (int)(u32)kb; kr[1] = (int)(u32)(kb >> 32); kr[2] = 8 * UNITS * SLOT; kr[3] = 0x00020000;
  u32 vo[4];
#pragma unroll
  for (int i = 0; i < 4; ++i) vo[i] = (lane >> 5) * 512 + (((lane & 31) ^ (((2 * i + (lane >> 5)) & 7) << 1)) << 4);
  const u32 lbase = (u32)(size_t)lds;
  {
    u32 lo = lbase, so = (u32)(j * UNITS * SLOT);
    asm volatile("s_waitcnt lgkmcnt(0)\n\t"
                 "s_barrier\n\t"
                 "s_cmp_lg_u32 %[w], 0\n\t"
                 "s_cbranch_scc1 .Lskip%=\n"
                 ".Lloop%=:\n\t"
                 "s_mov_b32 m0, %[lo]\n\t"
                 "s_add_u32 %[lo], %[lo], 0x1000\n\t"
                 "buffer_load_dwordx4 %[v0], %[r], %[so] offen nt lds\n\t"
                 "buffer_load_dwordx4 %[v1], %[r], %[so] offen offset:1024 nt lds\n\t"
                 "buffer_load_dwordx4 %[v2], %[r], %[so] offen offset:2048 nt lds\n\t"
                 "buffer_load_dwordx4 %[v3], %[r], %[so] offen offset:3072 nt lds\n\t"
                 "s_add_u32 %[so], %[so], 0x1000\n\t"
                 "s_cmp_lt_u32 %[lo], %[end]\n\t"
                 "s_cbranch_scc1 .Lloop%=\n"
                 ".Lskip%=:"
                 : [lo] "+s"(lo), [so] "+s"(so)
                 : [w] "s"(wave), [end] "s"(lbase + FIRSTS * SLOT), [v0] "v"(vo[0]), [v1] "v"(vo[1]), [v2] "v"(vo[2]), [v3] "v"(vo[3]), [r] "s"(kr),
                   "s"(x), "s"(wq), "s"(ws), "s"(vc)
                 : "memory", "scc");
  }

  if (wave == 0) {
    __builtin_amdgcn_s_setprio(3);
    const u64 vb = (u64)vc + (u64)pair * SK * (DH * 2);
    i32x4 vr;
    vr[0] = (int)(u32)vb; vr[1] = (int)(u32)(vb >> 32); vr[2] = 8 * UNITS * SLOT; vr[3] = 0x00020000;
    u32 pub = 0, freed = 0, p0 = 0;
    int pos = FIRSTS, s, total = UNITS + SV + 1;
#pragma unroll 1
    for (s = FIRSTS; s < total; ++s) {
      if (s == UNITS + SV) {
        const u32 pool = lds_wait<0>(&ctl[C_POOL], 1u);
        p0 = ((pool >> 8) & 0xff) - (UNITS + SV);
        total = UNITS + SV + (int)(pool & 0xff);
        if (s == total) break;
      }
      const u32 pp = p0 + s;
      const u32 so = (s < UNITS + SV ? (u32)(j * UNITS + (s & (UNITS - 1))) : (pp >> 3) * UNITS + SV + (pp & 7)) * SLOT;
      if (s >= NS && freed < (u32)(s - NS + 1)) {
        freed = free_min(ctl);
        if (freed < (u32)(s - NS + 1)) {
          asm volatile("s_waitcnt vmcnt(0)" ::: "memory");
          pub = s; ctl[C_FULL] = pub;
#pragma unroll 1
          for (u32 sp = 0; sp < SPIN_LDS; ++sp) { freed = free_min(ctl); if (freed >= (u32)(s - NS + 1)) break; __builtin_amdgcn_s_sleep(1); }
          MEMFENCE();
        }
      }
      i32x4 r = (s >= UNITS) ? vr : kr;
      const u32 lo = lbase + pos * SLOT;
#pragma unroll
      for (int q = 0; q < 4; ++q) {
        DMA_GROUP(lo + q * 4096, r, so + q * 4096, F - 1);
        const int landed = (16 * s + 4 * q + 5 - F) >> 4;
        if (landed > (int)pub) { pub = landed; ctl[C_FULL] = pub; }
      }
      pos = (pos + 1 == NS) ? 0 : pos + 1;
    }
    static_assert(F <= 32 && F > 16, "tail drain");
    asm volatile("s_waitcnt vmcnt(16)" ::: "memory");
    if ((int)pub < s - 1) ctl[C_FULL] = s - 1;
    asm volatile("s_waitcnt vmcnt(0)" ::: "memory");
    ctl[C_FULL] = (u32)s | FINAL;
    STAMP(2);
    __builtin_amdgcn_s_setprio(0);
  } else {
    u32x2 epoch;
    {
      const int r = lane >> 3, c = lane & 7;
      const int row0 = 64 * (wave - 1) + 8 * r;
      const u16* wp = wq + (size_t)row0 * NQ + kvh * 512 + j * 64 + c * 8;
      u32x4 w[8];
#pragma unroll
      for (int i = 0; i < 8; ++i) w[i] = *(const u32x4*)(wp + (size_t)i * NQ);
      const u32x4 xv = *(const u32x4*)(x + b * DM + row0);
      u32 eoff = WS_EPOCH + b * 256;
      asm volatile("" : "+v"(eoff));
      epoch = __builtin_amdgcn_raw_buffer_load_b64(wsr, eoff, 0, SC1);
      float a[8];
#pragma unroll
      for (int e = 0; e < 8; ++e) a[e] = 0.f;
#pragma unroll
      for (int i = 0; i < 4; ++i)
#pragma unroll
        for (int d = 0; d < 4; ++d) {
          a[2 * d] = dot2(pair_lo(w[2 * i][d], w[2 * i + 1][d]), xv[i], a[2 * d]);
          a[2 * d + 1] = dot2(pair_hi(w[2 * i][d], w[2 * i + 1][d]), xv[i], a[2 * d + 1]);
        }
      ((LAS float*)(lds + L_QRED))[(wave - 1) * 64 + 8 * c + r] = reduce8(a, lane);
      LGKM0();
      if (lane == 0) __hip_atomic_fetch_add((LAS u32*)&ctl[C_QCNT], 1u, __ATOMIC_RELAXED, __HIP_MEMORY_SCOPE_WORKGROUP);
    }
    const int n = lane & 15, g = lane >> 4;

    if (wave == 1) {
      __builtin_amdgcn_s_setprio(2);
      u64 z = ((u64)epoch[0] | ((u64)epoch[1] << 32)) + 0x9E3779B97F4A7C15ull;
      const u64 epoch_next = z;
      z = (z ^ (z >> 30)) * 0xBF58476D1CE4E5B9ull; z = (z ^ (z >> 27)) * 0x94D049BB133111EBull; z ^= z >> 31;
      const u32 tagA = __builtin_amdgcn_readfirstlane((u32)z);
      u32 tagB = __builtin_amdgcn_readfirstlane((u32)(z >> 32));
      if (tagB == tagA) tagB = ~tagA;
      if (lane == 0) {
        ctl[C_TAGA] = tagA; ctl[C_TAGB] = tagB;
        ctl[C_EP0] = (u32)epoch_next; ctl[C_EP1] = (u32)(epoch_next >> 32);
      }
      lds_wait<0>(&ctl[C_QCNT], 7u);
      STAMP(3);
      {
        float s = 0.f;
#pragma unroll
        for (int w = 0; w < 7; ++w) s += ((LAS float*)(lds + L_QRED))[w * 64 + lane];
        const u32 d = pk_bf16(s, dpp<QP_X1>(s));
        const u32 d2 = dppu<QP_X2>(d);
        put_granule(wsr, (lane & 3) == 0 ? WS_Q + (pair * 128 + j * 16 + (lane >> 2)) * 16 : OOB, d, d2, tagA, tagB);
        const u32 off[2] = {WS_Q + (pair * 128 + lane) * 16u, WS_Q + (pair * 128 + 64 + lane) * 16u};
        u32 d0[2], d1[2];
        gather<2, 1>(wsr, off, tagA, tagB, d0, d1);
#pragma unroll
        for (int i = 0; i < 2; ++i) *(LAS u32x2*)(lds + L_QS + (i * 64 + lane) * 8) = u32x2{d0[i], d1[i]};
        LGKM0();
      }
      STAMP(4);
      ctl[C_QRDY] = 1u;
      bf16x8 qf[8];
#pragma unroll
      for (int ks = 0; ks < 8; ++ks) qf[ks] = *(LAS bf16x8*)(lds + L_QS + (n & 1) * 512 + ks * 64 + g * 16);
      const u32 ka0 = n * 512 + ((g ^ ((n & 7) << 1)) << 4);
      int pos = 0;
      u32 full = 0;
#pragma unroll 1
      for (int u = 0; u < UNITS; ++u) {
        if (full < (u32)(u + 1)) full = lds_wait<1>(&ctl[C_FULL], u + 1);
        MEMFENCE();
        LAS char* kbase = lds + pos * SLOT;
        pos = (pos + 1 == NS) ? 0 : pos + 1;
        bf16x8 k0[8], k1[8];
#pragma unroll
        for (int ks = 0; ks < 8; ++ks) {
          const u32 a = (ka0 ^ ((ks & 3) << 6)) + (ks >> 2) * 256;
          k0[ks] = *(LAS bf16x8*)(kbase + a);
          k1[ks] = *(LAS bf16x8*)(kbase + a + 8192);
        }
        MEMFENCE();
        if (lane < 4) ctl[C_FREE + lane] = u + 1;
        SCHED();
        f32x4 s0 = {0.f, 0.f, 0.f, 0.f}, s1 = {0.f, 0.f, 0.f, 0.f};
#pragma unroll
        for (int ks = 0; ks < 8; ++ks) {
          s0 = __builtin_amdgcn_mfma_f32_16x16x32_bf16(k0[ks], qf[ks], s0, 0, 0, 0);
          s1 = __builtin_amdgcn_mfma_f32_16x16x32_bf16(k1[ks], qf[ks], s1, 0, 0, 0);
        }
        const u32x4 sw = {pk_bf16(s0[0], s0[1]), pk_bf16(s0[2], s0[3]), pk_bf16(s1[0], s1[1]), pk_bf16(s1[2], s1[3])};
        if (n < 2) *(LAS u32x4*)(lds + L_SB + u * 128 + g * 32 + n * 16) = sw;
      }
      STAMP(1);
      constexpr float C = 0.0625f * 1.4426950408889634f;
      LGKM0();
      u32x4 sv[4];
#pragma unroll
      for (int i = 0; i < 4; ++i) sv[i] = *(LAS u32x4*)(lds + L_SB + (i * 64 + lane) * 16);
      __builtin_amdgcn_raw_buffer_store_b128(sv[3], wsr, WS_S + (pair * 8 + j) * 1024 + lane * 16, 0, SC1);
      float mx = -INFINITY;
#pragma unroll
      for (int i = 0; i < 4; ++i)
#pragma unroll
        for (int e = 0; e < 4; ++e) mx = fmaxf(mx, fmaxf(bf_lo(sv[i][e]), bf_hi(sv[i][e])));
      mx = fmaxf(mx, dpp<QP_X2>(mx)); mx = fmaxf(mx, dpp<ROR4>(mx)); mx = fmaxf(mx, dpp<ROR8>(mx));
      const float mc = xrow_max(mx) * C;
      float ls = 0.f;
#pragma unroll
      for (int i = 0; i < 4; ++i) {
#pragma unroll
        for (int e = 0; e < 4; ++e) {
          const float p0 = __builtin_amdgcn_exp2f(fmaf(bf_lo(sv[i][e]), C, -mc)), p1 = __builtin_amdgcn_exp2f(fmaf(bf_hi(sv[i][e]), C, -mc));
          ls += p0 + p1;
          sv[i][e] = pk_bf16(p0, p1);
        }
        if (i < 3) *(LAS u32x4*)(lds + L_SB + (i * 64 + lane) * 16) = sv[i];
      }
      LGKM0();
      ctl[C_PRDY] = 1u;
      ls += dpp<QP_X2>(ls); ls += dpp<ROR4>(ls); ls += dpp<ROR8>(ls);
      ls = swap16_add(ls, ls); ls = swap32_add(ls, ls);
      u32 ca = lds_ld(&ctl[C_FULL]) + 1;
      ca += (ca ^ CKB) & 1;
      ca = lds_wait<1>(&ctl[C_FULL], ca);
      const u32 ta = (u32)__builtin_amdgcn_s_memrealtime();
      lds_wait<8>(&ctl[C_FULL], UNITS + CKB - 1);
      const u32 cb = lds_wait<1>(&ctl[C_FULL], min(max(ca + 2, (u32)(UNITS + CKB)), (u32)(UNITS + SV - 3)));
      const u32 tb = (u32)__builtin_amdgcn_s_memrealtime();
      const float pace = (float)(tb - ta) / (float)(cb - ca);
      const u32 arrive = tb + (u32)(int)(pace * (float)(int)(UNITS + CKB - cb));
      asm volatile("s_waitcnt vmcnt(0)" ::: "memory");
      put_granule(wsr, lane < 3 ? WS_ML + ((pair * 8 + j) * 4 + lane) * 16 : OOB, lane < 2 ? __float_as_uint(mc) : arrive,
                  __float_as_uint(lane < 2 ? ls : pace), tagA, tagB);
      lds_wait<8>(&ctl[C_FULL], UNITS + CKG);
      u32 d0[1], d1[1];
      {
        const u32 off[1] = {(lane & 3) < 3 ? WS_ML + (pair * 32 + (lane & 31)) * 16u : OOB};
        gather<1, 8>(wsr, off, tagA, tagB, d0, d1);
      }
      STAMP(10);
      u32 first, count;
      {
        float av[8], rv[8], sr = 0.f, sa = 0.f;
        const u32 a0 = __builtin_amdgcn_readlane(d0[0], 2);
        float mean = 0.f;
#pragma unroll
        for (int i = 0; i < 8; ++i) { rv[i] = __uint_as_float(__builtin_amdgcn_readlane(d1[0], 4 * i + 2)); mean += 0.125f * rv[i]; }
#pragma unroll
        for (int i = 0; i < 8; ++i) {
          const float pc = mean + SHR * (rv[i] - mean);
          av[i] = (float)(int)(__builtin_amdgcn_readlane(d0[0], 4 * i + 2) - a0) + pc * (float)(SV - CKB);
          rv[i] = 1.f / pc;
          sr += rv[i]; sa += av[i] * rv[i];
        }
        const float E = ((float)NPOOL + sa) / sr;
        float tot = 0.f;
#pragma unroll
        for (int i = 0; i < 8; ++i) { av[i] = fmaxf((E - av[i]) * rv[i], 0.f); tot += av[i]; }
        const float scale = (float)NPOOL / tot;
        float cum = 0.f;
        int prev = 0, lo = 0, hi = 0;
        bool ok = BAL;
#pragma unroll
        for (int i = 0; i < 8; ++i) {
          cum += av[i];
          const int c = i == 7 ? NPOOL : (int)(cum * scale + 0.5f);
          ok = ok && c >= prev && c - prev <= NMAX && c <= NPOOL;
          if (i == j) { lo = prev; hi = c; }
          prev = c;
        }
        first = __builtin_amdgcn_readfirstlane(ok ? lo : 8 * j);
        count = __builtin_amdgcn_readfirstlane(ok ? hi - lo : 8);
      }
      ctl[C_POOL] = 0x10000u | (first << 8) | count;
      u32x4 sp[3];
#pragma unroll
      for (int i = 0; i < 3; ++i)
        sp[i] = __builtin_amdgcn_raw_buffer_load_b128(wsr, (u32)(i * 64 + lane) < count * 8 ? WS_S + pair * 8192 + first * 128 + (i * 64 + lane) * 16 : OOB, 0, SC1);
      const float ms = (lane & 2) ? -INFINITY : __uint_as_float(d0[0]);
      float M = fmaxf(ms, dpp<QP_X2>(ms)); M = fmaxf(M, dpp<ROR4>(M)); M = fmaxf(M, dpp<ROR8>(M)); M = xrow_max(M);
      float L = (lane & 2) ? 0.f : __builtin_amdgcn_exp2f(ms - M) * __uint_as_float(d1[0]);
      L += dpp<QP_X2>(L); L += dpp<ROR4>(L); L += dpp<ROR8>(L); L = swap16_add(L, L);
      const float il = 1.f / L;
      const float sc = __builtin_amdgcn_exp2f(mc - M) * il;
      if (lane < 2) { ctl[C_SC0 + lane] = __float_as_uint(sc); ctl[C_IL0 + lane] = __float_as_uint(il); }
#pragma unroll 1
      for (u32 spn = 0; spn < SPIN_LDS && free_min(ctl) < (u32)(UNITS + NMAX - 8); ++spn) __builtin_amdgcn_s_sleep(2);
      MEMFENCE();
#pragma unroll
      for (int i = 0; i < 3; ++i) {
#pragma unroll
        for (int e = 0; e < 4; ++e)
          sp[i][e] = pk_bf16(__builtin_amdgcn_exp2f(fmaf(bf_lo(sp[i][e]), C, -M)), __builtin_amdgcn_exp2f(fmaf(bf_hi(sp[i][e]), C, -M)));
        if ((u32)(i * 64 + lane) < count * 8) *(LAS u32x4*)(lds + L_SB + ((SV * 8 + i * 64 + lane) & 255) * 16) = sp[i];
      }
      LGKM0();
      ctl[C_CRDY] = 1u;
      __builtin_amdgcn_s_setprio(0);
    } else {
      lds_wait<8>(&ctl[C_QRDY], 1u);
      const u32 tagA = lds_ld(&ctl[C_TAGA]), tagB = lds_ld(&ctl[C_TAGB]);

      if (wave <= 5) {
        const int cw = wave - 2;
        const int c8 = 16 * cw + n;
        const bool dlive = c8 < 2 * NDEST;
        const u32 gofs = dlive ? ((u32)(b * NDEST + (c8 >> 1)) * 512 + sidx * 8 + (c8 & 1) * 4 + (g >> 1) + 2 * (g & 1)) * 16 : OOB;
        const u32 wofs = dlive ? (u32)(4 * g * DM + c8 * 8) * 2 : OOB;
        float f0 = 0.f, f1 = 0.f;
        if (sidx < NFFN) {
          const __amdgpu_buffer_rsrc_t r2 = __builtin_amdgcn_make_buffer_rsrc((void*)(w2 + (size_t)(sidx * 32) * DM), 0, 32 * DM * 2, 0x00020000);
          u32x4 ua[4], ub[4];
#pragma unroll
          for (int d = 0; d < 4; ++d) {
            ua[d] = __builtin_amdgcn_raw_buffer_load_b128(r2, wofs, d * DM * 2, 0); PACE();
            ub[d] = __builtin_amdgcn_raw_buffer_load_b128(r2, wofs, (d + 16) * DM * 2, 0); PACE();
          }
          lds_wait<8>(&ctl[C_HRDY], 1u);
          const u32x4 hv = *(LAS u32x4*)(lds + L_HS + g * 16);
          float y[8];
#pragma unroll
          for (int e = 0; e < 8; ++e) y[e] = 0.f;
#pragma unroll
          for (int d = 0; d < 4; ++d)
#pragma unroll
            for (int e2 = 0; e2 < 4; ++e2) {
              y[2 * e2] = dot2(pair_lo(ua[d][e2], ub[d][e2]), hv[d], y[2 * e2]);
              y[2 * e2 + 1] = dot2(pair_hi(ua[d][e2], ub[d][e2]), hv[d], y[2 * e2 + 1]);
            }
          float y4[4];
#pragma unroll
          for (int e = 0; e < 4; ++e) y4[e] = swap32_add(y[(e & 1) + 4 * (e >> 1)], y[(e & 1) + 4 * (e >> 1) + 2]);
          f0 = swap16_add(y4[0], y4[2]); f1 = swap16_add(y4[1], y4[3]);
        }
        lds_wait<8>(&ctl[C_GRDY], 1u);
        const float gate = __uint_as_float(lds_ld(&ctl[C_GATE]));
        f0 *= rbf(1.f - gate); f1 *= rbf(1.f - gate);
        u32 wr[2][4][8];
        {
          const __amdgpu_buffer_rsrc_t ro = __builtin_amdgcn_make_buffer_rsrc((void*)(wo + (size_t)(kvh * 512 + j * 64) * DM), 0, 64 * DM * 2, 0x00020000);
          u32x4 ua[2][4], ub[2][4];
#pragma unroll
          for (int ks = 0; ks < 2; ++ks)
#pragma unroll
            for (int d = 0; d < 4; ++d) {
              ua[ks][d] = __builtin_amdgcn_raw_buffer_load_b128(ro, wofs, (32 * ks + d) * DM * 2, 0); PACE();
              ub[ks][d] = __builtin_amdgcn_raw_buffer_load_b128(ro, wofs, (32 * ks + d + 16) * DM * 2, 0); PACE();
            }
#pragma unroll
          for (int ks = 0; ks < 2; ++ks)
#pragma unroll
            for (int d = 0; d < 4; ++d)
#pragma unroll
              for (int e2 = 0; e2 < 4; ++e2) {
                wr[ks][d][2 * e2] = pair_lo(ua[ks][d][e2], ub[ks][d][e2]);
                wr[ks][d][2 * e2 + 1] = pair_hi(ua[ks][d][e2], ub[ks][d][e2]);
                asm volatile("" : "+v"(wr[ks][d][2 * e2]), "+v"(wr[ks][d][2 * e2 + 1]));
              }
        }
        const int vq = n >> 2, vp = n & 3, vrow = 4 * g + vq;
        const u32 va0 = ((vrow * 512 + (((vp >> 1) ^ ((vrow & 7) << 1)) << 4) + (vp & 1) * 8) ^ ((cw & 1) << 7)) + (cw >> 1) * 256;
        f32x4 acc[4];
        float st0 = 0.f, st1 = 0.f, il = 0.f;
#pragma unroll
        for (int t = 0; t < 4; ++t) acc[t] = f32x4{0.f, 0.f, 0.f, 0.f};
        lds_wait<8>(&ctl[C_PRDY], 1u);
        __builtin_amdgcn_s_setprio(1);
        int pos = UNITS % NS;
        u32 full = 0;
        auto pick = [&](int e) {
          const float a0 = (g & 1) ? acc[e][1] : acc[e][0], a2 = (g & 1) ? acc[2 + e][1] : acc[2 + e][0];
          return (g & 2) ? a2 : a0;
        };
        auto pv_step = [&](int u) -> bool {
          if (u == SV) {
            lds_wait<1>(&ctl[C_CRDY], 1u);
            const float sc = __uint_as_float(ctl[C_SC0 + (g & 1)]);
            il = __uint_as_float(ctl[C_IL0 + (g & 1)]);
            st0 = pick(0) * sc; st1 = pick(1) * sc;
#pragma unroll
            for (int t = 0; t < 4; ++t) acc[t] = f32x4{0.f, 0.f, 0.f, 0.f};
          }
          if (full < (u32)(UNITS + u + 1)) full = lds_wait<1>(&ctl[C_FULL], UNITS + u + 1);
          if ((full & ~FINAL) < (u32)(UNITS + u + 1)) return false;
          MEMFENCE();
          const bf16x8 pf = *(LAS bf16x8*)(lds + L_SB + (u & 31) * 128 + g * 32 + (n & 1) * 16);
          LAS char* vbase = lds + pos * SLOT;
          pos = (pos + 1 == NS) ? 0 : pos + 1;
          s16x4 lo[4], hi[4];
#pragma unroll
          for (int t = 0; t < 4; ++t) {
            const u32 a = va0 ^ (t << 5);
            lo[t] = __builtin_amdgcn_ds_read_tr16_b64_v4i16((LAS s16x4*)(vbase + a));
            hi[t] = __builtin_amdgcn_ds_read_tr16_b64_v4i16((LAS s16x4*)(vbase + a + 8192));
          }
          MEMFENCE();
          ctl[C_FREE + cw] = UNITS + u + 1;
          SCHED();
#pragma unroll
          for (int t = 0; t < 4; ++t) {
            const s16x8 vv = __builtin_shufflevector(lo[t], hi[t], 0, 1, 2, 3, 4, 5, 6, 7);
            acc[t] = __builtin_amdgcn_mfma_f32_16x16x32_bf16(pf, __builtin_bit_cast(bf16x8, vv), acc[t], 0, 0, 0);
          }
          return true;
        };
#pragma unroll 1
        for (int u = 0; u < SV + NMAX; ++u)
          if (!pv_step(u)) break;
        const u32 pin = WS_P + ((pair * 8 + j) * 256 + cw * 64 + lane) * 16u;
        put_granule(wsr, WS_P + ((((pair * 8 + (g & 1) * 4 + cw) * 8 + j) * 32) + (g >> 1) * 16 + n) * 16,
                    __float_as_uint(fmaf(pick(0), il, st0)), __float_as_uint(fmaf(pick(1), il, st1)), tagA, tagB);
        if (wave == 2) STAMP(5);
        {
          const u32 off[1] = {pin};
          u32 d0[1], d1[1];
          gather<1, 0>(wsr, off, tagA, tagB, d0, d1);
          const float o0 = swap32_add(__uint_as_float(d0[0]), __uint_as_float(d0[0])), o1 = swap32_add(__uint_as_float(d1[0]), __uint_as_float(d1[0]));
          *(LAS f32x2*)(lds + L_PA + cw * 256 + (lane & 31) * 8) = f32x2{o0, o1};
          MEMFENCE();
          if (lane == 0) __hip_atomic_fetch_add((LAS u32*)&ctl[C_OCNT], 1u, __ATOMIC_RELAXED, __HIP_MEMORY_SCOPE_WORKGROUP);
          lds_wait<0>(&ctl[C_OCNT], 4u);
          if (wave == 2) STAMP(6);
          f32x2 pa[4];
#pragma unroll
          for (int c = 0; c < 4; ++c) pa[c] = *(LAS f32x2*)(lds + L_PA + c * 256 + (lane & 31) * 8);
          const f32x2 os = (pa[0] + pa[1]) + (pa[2] + pa[3]);
          ((LAS u32*)(lds + L_OS + cw * 128))[lane & 31] = pk_bf16(os[0], os[1]);
          MEMFENCE();
        }
        const u32x4 ov0 = *(LAS u32x4*)(lds + L_OS + cw * 128 + g * 16), ov1 = *(LAS u32x4*)(lds + L_OS + cw * 128 + 64 + g * 16);
        float y[8];
#pragma unroll
        for (int e = 0; e < 8; ++e) y[e] = 0.f;
#pragma unroll
        for (int d = 0; d < 4; ++d)
#pragma unroll
          for (int e = 0; e < 8; ++e) y[e] = dot2(wr[0][d][e], ov0[d], y[e]);
#pragma unroll
        for (int d = 0; d < 4; ++d)
#pragma unroll
          for (int e = 0; e < 8; ++e) y[e] = dot2(wr[1][d][e], ov1[d], y[e]);
        float y4[4];
#pragma unroll
        for (int e = 0; e < 4; ++e) y4[e] = swap32_add(y[(e & 1) + 4 * (e >> 1)], y[(e & 1) + 4 * (e >> 1) + 2]);
        put_granule(wsr, WS_Y + gofs, __float_as_uint(fmaf(gate, swap16_add(y4[0], y4[2]), f0)), __float_as_uint(fmaf(gate, swap16_add(y4[1], y4[3]), f1)), tagA, tagB);
        if (wave == 2) STAMP(7);
      } else if (wave == 7) {
        if (sidx < NFFN) {
          const int r = lane >> 2, c = lane & 3;
          const u16* wp = w1 + (size_t)(28 * r) * DFF + sidx * 32 + c * 8;
          u32x4 w[28];
#pragma unroll
          for (int i = 0; i < 28; ++i) { w[i] = *(const u32x4*)(wp + (size_t)i * DFF); if ((i & 3) == 3) PACE(); }
          u32x2 xv[7];
#pragma unroll
          for (int i = 0; i < 7; ++i) xv[i] = *(const u32x2*)(x + b * DM + 28 * r + 4 * i);
          float a[8];
#pragma unroll
          for (int e = 0; e < 8; ++e) a[e] = 0.f;
#pragma unroll
          for (int i = 0; i < 14; ++i)
#pragma unroll
            for (int d = 0; d < 4; ++d) {
              a[2 * d] = dot2(pair_lo(w[2 * i][d], w[2 * i + 1][d]), xv[i >> 1][i & 1], a[2 * d]);
              a[2 * d + 1] = dot2(pair_hi(w[2 * i][d], w[2 * i + 1][d]), xv[i >> 1][i & 1], a[2 * d + 1]);
            }
          float dsum = reduce8(a, lane);
          dsum += __shfl_xor(dsum, 4);
          const float hv = fmaxf(rbf(dsum), 0.f);
          const int unit = 8 * c + (lane >> 3);
          ((LAS u16*)(lds + L_HS))[2 * (unit & 15) + (unit >> 4)] = (u16)(__float_as_uint(hv) >> 16);
          LGKM0();
          ctl[C_HRDY] = 1u;
        }
      } else {
        {
          const u32x4 xv = *(const u32x4*)(x + b * DM + (lane < 56 ? lane : 0) * 8), gv = *(const u32x4*)(gw + (lane < 56 ? lane : 0) * 8);
          float gsum = 0.f;
#pragma unroll
          for (int i = 0; i < 4; ++i) gsum += rbf(bf_lo(xv[i]) * bf_lo(gv[i])) + rbf(bf_hi(xv[i]) * bf_hi(gv[i]));
          gsum = lane < 56 ? gsum : 0.f;
#pragma unroll
          for (int o = 1; o < 64; o <<= 1) gsum += __shfl_xor(gsum, o);
          ctl[C_GATE] = __float_as_uint(rbf(1.f / (1.f + __expf(-rbf(gsum)))));
          LGKM0();
          ctl[C_GRDY] = 1u;
        }
      }
    }
  }

  if (didx >= NDEST) return;
  {
    const u32 tagA = lds_ld(&ctl[C_TAGA]), tagB = lds_ld(&ctl[C_TAGB]);
    lds_wait<4>(&ctl[C_FULL], FINAL);
    lds_wait<1>(&ctl[C_OCNT], 4u);
    const u32 off[1] = {WS_Y + yin};
    u32 d0[1], d1[1];
    gather<1, 0>(wsr, off, tagA, tagB, d0, d1);
    const float z = inbox_sum(__uint_as_float(d0[0]), __uint_as_float(d1[0]));
    ((LAS float*)(lds + L_PY))[wave * 16 + 2 * (lane & 7) + (lane >> 5)] = z;
    MEMFENCE();
    if (lds_inc(&ctl[C_YCNT], lane) == 7u) {
      const int c = lane & 15;
      float p[8];
#pragma unroll
      for (int w = 0; w < 8; ++w) p[w] = ((LAS float*)(lds + L_PY))[w * 16 + c];
      const float res = ((p[0] + p[1]) + (p[2] + p[3])) + ((p[4] + p[5]) + (p[6] + p[7]));
      if (lane < 16) out[b * DM + didx * 16 + c] = __builtin_bit_cast(u16, (__bf16)res);
      STAMP(8);
      if (didx == 0 && lane == 0) {
        const u64 en = (u64)ctl[C_EP0] | ((u64)ctl[C_EP1] << 32);
        __hip_atomic_store((GAS u64*)(ws + WS_EPOCH + b * 256), en, __ATOMIC_RELAXED, __HIP_MEMORY_SCOPE_AGENT);
      }
    }
  }
}

extern "C" void kernel_launch(void* const* d_in, const int* in_sizes, int n_in, void* d_out, int out_size, void* d_ws, size_t ws_size, hipStream_t stream) {
  fused_kernel<<<256, 512, 0, stream>>>((const u16*)d_in[0], (const u16*)d_in[1], (const u16*)d_in[2], (const u16*)d_in[3], (const u16*)d_in[4],
                                        (const u16*)d_in[6], (const u16*)d_in[7], (const u16*)d_in[8], (u16*)d_out, (char*)d_ws);
}
```

```cpp
#include <hip/hip_runtime.h>
#include <stdint.h>

typedef unsigned short u16;
typedef unsigned u32;
typedef unsigned long long u64;
typedef u32 u32x2 __attribute__((ext_vector_type(2)));
typedef u32 u32x4 __attribute__((ext_vector_type(4)));
typedef int i32x4 __attribute__((ext_vector_type(4)));
typedef float f32x2 __attribute__((ext_vector_type(2)));
typedef float f32x4 __attribute__((ext_vector_type(4)));
typedef unsigned short u16x2 __attribute__((ext_vector_type(2)));
typedef short s16x4 __attribute__((ext_vector_type(4)));
typedef short s16x8 __attribute__((ext_vector_type(8)));
typedef __bf16 bf16x2 __attribute__((ext_vector_type(2)));
typedef __bf16 bf16x8 __attribute__((ext_vector_type(8)));
#define LAS __attribute__((address_space(3)))

constexpr int DM = 448, NQ = 4096, SK = 8192, DH = 256, DFF = 1792;
constexpr int NS = 9;
constexpr int SLOT = 16384;
constexpr int F = 22;
constexpr int UNITS = 32;
constexpr int SV = 24;
constexpr int NPOOL = 8 * (UNITS - SV);
constexpr int CKV = 6;
constexpr int CKB = 10, CKG = 19;
constexpr float SHR = 0.3f;
constexpr int KREG = 2;
constexpr int T_TOUCH = 20, T_WQ = 160, D_WQ = 60, T_W1 = 910, D_W1 = 22;
constexpr int CODEB = 23296;
constexpr int NMAX = 20;
constexpr u32 FINAL = 0x80000000u;
constexpr int NFFN = 56;
constexpr int NDEST = 28;

constexpr u32 WS_EPOCH = 0;
constexpr u32 WS_Q = 4096;
constexpr u32 WS_ML = WS_Q + 32 * 128 * 16;
constexpr u32 WS_P = WS_ML + 32 * 8 * 4 * 16;
constexpr u32 WS_Y = WS_P + 32 * 8 * 8 * 32 * 16;
constexpr u32 WS_S = WS_Y + 4 * NDEST * 8192;
constexpr u32 WS_END = WS_S + 32 * 64 * 128;

constexpr int L_CTL = NS * SLOT;
constexpr int L_QRED = L_CTL + 256;
constexpr int L_QS = L_QRED + 7 * 256;
constexpr int L_HS = L_QS + 1024;
constexpr int L_SB = L_HS + 64;
constexpr int L_PA = L_SB + UNITS * 128;
constexpr int L_PY = L_PA + 512;
constexpr int L_FB = L_PY + 512;
constexpr int L_END = L_FB + 2048;
static_assert(L_END <= 163840 && L_SB % 16 == 0, "LDS");
enum { C_FREE = 0  , C_FULL = 4, C_QCNT = 5, C_QRDY = 6, C_HRDY = 7, C_OCNT = 8, C_PRDY = 9, C_CRDY = 10, C_YCNT = 11,
       C_GRDY = 12, C_POOL = 13, C_FREE1 = 14, C_SCNT = 15,
       C_TAGA = 16, C_TAGB = 17, C_EP0 = 18, C_EP1 = 19, C_SC0 = 20, C_SC1 = 21, C_IL0 = 22, C_IL1 = 23, C_GATE = 24, C_LAST = 25, C_VMAX = 26, C_GRID = 27 };

constexpr u32 OOB = 0x7ffffff0u;
constexpr int SC1 = 16;
constexpr u32 spin_cap(int sleep) { return (1u << 28) / (64u * sleep + 128u); }
constexpr u32 SPIN_MEM = 1u << 18;

#define MEMFENCE() asm volatile("" ::: "memory")
#define LGKM0() asm volatile("s_waitcnt lgkmcnt(0)" ::: "memory")
#define SCHED() __builtin_amdgcn_sched_barrier(0)

__device__ __forceinline__ float bf_lo(u32 v) { return __uint_as_float(v << 16); }
__device__ __forceinline__ float bf_hi(u32 v) { return __uint_as_float(v & 0xffff0000u); }
__device__ __forceinline__ u32 pk_bf16(float lo, float hi) {
  bf16x2 r; r[0] = (__bf16)lo; r[1] = (__bf16)hi; return __builtin_bit_cast(u32, r);
}
__device__ __forceinline__ float rbf(float f) { return bf_lo((u32)__builtin_bit_cast(u16, (__bf16)f)); }
__device__ __forceinline__ float dot2(u32 a, u32 b, float acc) {
  return __builtin_amdgcn_fdot2_f32_bf16(__builtin_bit_cast(bf16x2, a), __builtin_bit_cast(bf16x2, b), acc, false);
}
__device__ __forceinline__ u32 pair_lo(u32 a, u32 b) { return __builtin_amdgcn_perm(b, a, 0x05040100u); }
__device__ __forceinline__ u32 pair_hi(u32 a, u32 b) { return __builtin_amdgcn_perm(b, a, 0x07060302u); }
template <int CTRL> __device__ __forceinline__ float dpp(float x) {
  return __builtin_bit_cast(float, __builtin_amdgcn_mov_dpp(__builtin_bit_cast(int, x), CTRL, 0xf, 0xf, true));
}
template <int CTRL> __device__ __forceinline__ u32 dppu(u32 x) { return (u32)__builtin_amdgcn_mov_dpp((int)x, CTRL, 0xf, 0xf, true); }
constexpr int QP_X1 = 0xB1, QP_X2 = 0x4E, ROR4 = 0x124, ROR8 = 0x128;
__device__ __forceinline__ float swap32_add(float a, float b) {
  auto s = __builtin_amdgcn_permlane32_swap(__float_as_uint(a), __float_as_uint(b), false, false);
  return __uint_as_float(s[0]) + __uint_as_float(s[1]);
}
__device__ __forceinline__ float swap16_add(float a, float b) {
  auto s = __builtin_amdgcn_permlane16_swap(__float_as_uint(a), __float_as_uint(b), false, false);
  return __uint_as_float(s[0]) + __uint_as_float(s[1]);
}
__device__ __forceinline__ float xrow_max(float x) {
  auto s = __builtin_amdgcn_permlane16_swap(__float_as_uint(x), __float_as_uint(x), false, false);
  x = fmaxf(__uint_as_float(s[0]), __uint_as_float(s[1]));
  auto t = __builtin_amdgcn_permlane32_swap(__float_as_uint(x), __float_as_uint(x), false, false);
  return fmaxf(__uint_as_float(t[0]), __uint_as_float(t[1]));
}
__device__ __forceinline__ float reduce8(const float (&a)[8], int lane) {
  float b4[4], c2[2];
#pragma unroll
  for (int e = 0; e < 4; ++e) b4[e] = swap32_add(a[e], a[e + 4]);
#pragma unroll
  for (int e = 0; e < 2; ++e) c2[e] = swap16_add(b4[e], b4[e + 2]);
  const float t0 = c2[0] + dpp<ROR8>(c2[0]), t1 = c2[1] + dpp<ROR8>(c2[1]);
  return (lane & 8) ? t1 : t0;
}

__device__ __forceinline__ u32 lds_ld(volatile LAS u32* p) { return __builtin_amdgcn_readfirstlane(*p); }
template <int SLEEP>
__device__ __forceinline__ u32 lds_wait(volatile LAS u32* p, u32 want) {
  u32 v = 0;
#pragma unroll 1
  for (u32 sp = 0; sp < spin_cap(SLEEP); ++sp) {
    v = lds_ld(p);
    if (v >= want) break;
    if (SLEEP) __builtin_amdgcn_s_sleep(SLEEP);
  }
  MEMFENCE();
  return v;
}
__device__ __forceinline__ void gate_at(u32 t0, u32 ticks) {
  SCHED(); MEMFENCE();
#pragma unroll 1
  for (u32 sp = 0; sp < spin_cap(2) && (u32)__builtin_amdgcn_s_memrealtime() - t0 < ticks; ++sp) __builtin_amdgcn_s_sleep(2);
  MEMFENCE(); SCHED();
}
__device__ __forceinline__ u32 free_min(volatile LAS u32* ctl) {
  const u32x4 v = *(volatile LAS u32x4*)(ctl + C_FREE);
  const u32 w = ctl[C_FREE1];
  return __builtin_amdgcn_readfirstlane(min(min(min(v[0], v[1]), min(v[2], v[3])), w));
}
__device__ __forceinline__ u32 lds_inc(volatile LAS u32* p, int lane) {
  u32 old = 0;
  if (lane == 0) old = __hip_atomic_fetch_add((LAS u32*)p, 1u, __ATOMIC_RELAXED, __HIP_MEMORY_SCOPE_WORKGROUP);
  old = __builtin_amdgcn_readfirstlane(old);
  MEMFENCE();
  return old;
}

template <int N, int SLEEP>
__device__ __forceinline__ void gather(__amdgpu_buffer_rsrc_t ws, const u32 (&off)[N], u32 tagA, u32 tagB, u32 (&d0)[N], u32 (&d1)[N]) {
  u32 o[N];
#pragma unroll
  for (int i = 0; i < N; ++i) { o[i] = off[i]; d0[i] = 0u; d1[i] = 0u; }
#pragma unroll 1
  for (u32 sp = 0; sp < SPIN_MEM; ++sp) {
    u32x4 t[N];
    MEMFENCE();
#pragma unroll
    for (int i = 0; i < N; ++i) t[i] = __builtin_amdgcn_raw_buffer_load_b128(ws, o[i], 0, SC1);
    bool all = true;
#pragma unroll
    for (int i = 0; i < N; ++i) {
      const bool g = t[i][1] == tagA && t[i][3] == tagB;
      d0[i] = g ? t[i][0] : d0[i]; d1[i] = g ? t[i][2] : d1[i];
      o[i] = g ? OOB : o[i];
      all = all && o[i] >= OOB;
    }
    if (__all(all)) break;
    if (SLEEP) __builtin_amdgcn_s_sleep(SLEEP);
  }
  MEMFENCE();
}
__device__ __forceinline__ void put_granule(__amdgpu_buffer_rsrc_t ws, u32 off, u32 d0, u32 d1, u32 tagA, u32 tagB) {
  u32x4 v = {d0, tagA, d1, tagB};
  __builtin_amdgcn_raw_buffer_store_b128(v, ws, off, 0, SC1);
}
__device__ __forceinline__ float inbox_sum(float y0, float y1) {
  float z = swap32_add(y0, y1);
  z = swap16_add(z, z);
  z += dpp<ROR8>(z);
  return z;
}

__global__ void __launch_bounds__(512, 2)
fused_kernel(const u16* __restrict__ kc, const u16* __restrict__ wq, const u16* __restrict__ x, char* ws, const u16* __restrict__ vc,
             u16* __restrict__ out, const u16* __restrict__ wo, const u16* __restrict__ w2, const u16* __restrict__ w1, const u16* __restrict__ gw) {
  __shared__ __attribute__((aligned(16))) char lds_raw[L_END];
  static_assert(C_FREE == 0 && C_FREE1 == 14 && KREG == 2 && UNITS * SLOT == 1 << 19 && CODEB <= 32 * 768, "prologue constants");
  u32 vo[4];
  asm volatile("v_readfirstlane_b32 s5, %[tid]\n\t"
               "s_mov_b32 s11, 0x20000\n\t"
               "s_cmp_ge_u32 s5, 64\n\t"
               "s_cbranch_scc1 .Lrest%=\n\t"
               "s_load_dwordx2 s[8:9], %[ka], 0x0\n\t"
               "s_lshl_b32 s6, %[bid], 19\n\t"
               "v_lshrrev_b32 %[v1], 5, %[tid]\n\t"
               "v_and_b32 %[v0], 31, %[tid]\n\t"
               "v_lshlrev_b32 %[v2], 1, %[v1]\n\t"
               "v_xor_b32 %[v0], %[v0], %[v2]\n\t"
               "v_lshlrev_b32 %[v0], 4, %[v0]\n\t"
               "v_lshl_or_b32 %[v0], %[v1], 9, %[v0]\n\t"
               "v_xor_b32 %[v1], 64, %[v0]\n\t"
               "v_xor_b32 %[v2], 0x80, %[v0]\n\t"
               "v_xor_b32 %[v3], 0xc0, %[v0]\n\t"
               "s_mov_b32 s10, 0xc000\n\t"
               "s_mov_b32 s7, 0\n\t"
               "s_waitcnt lgkmcnt(0)\n\t"
               "s_add_u32 s8, s8, s6\n\t"
               "s_addc_u32 s9, s9, 0\n"
               ".La%=:\n\t"
               "s_mov_b32 m0, s7\n\t"
               "s_nop 0\n\t"
               "buffer_load_dwordx4 %[v0], s[8:11], s7 offen nt lds\n\t"
               "buffer_load_dwordx4 %[v1], s[8:11], s7 offen offset:1024 nt lds\n\t"
               "buffer_load_dwordx4 %[v2], s[8:11], s7 offen offset:2048 nt lds\n\t"
               "buffer_load_dwordx4 %[v3], s[8:11], s7 offen offset:3072 nt lds\n\t"
               "s_add_u32 s7, s7, 0x1000\n\t"
               "s_cmp_lt_u32 s7, 0x4000\n\t"
               "s_cbranch_scc1 .La%=\n\t"
               "s_barrier\n"
               ".Lb%=:\n\t"
               "s_mov_b32 m0, s7\n\t"
               "s_nop 0\n\t"
               "buffer_load_dwordx4 %[v0], s[8:11], s7 offen nt lds\n\t" "s_waitcnt vmcnt(%[fw])\n\t"
               "buffer_load_dwordx4 %[v1], s[8:11], s7 offen offset:1024 nt lds\n\t" "s_waitcnt vmcnt(%[fw])\n\t"
               "buffer_load_dwordx4 %[v2], s[8:11], s7 offen offset:2048 nt lds\n\t" "s_waitcnt vmcnt(%[fw])\n\t"
               "buffer_load_dwordx4 %[v3], s[8:11], s7 offen offset:3072 nt lds\n\t" "s_waitcnt vmcnt(%[fw])\n\t"
               "s_add_u32 s7, s7, 0x1000\n\t"
               "s_cmp_lt_u32 s7, %[e2]\n\t"
               "s_cbranch_scc1 .Lb%=\n"
               ".Lc%=:\n\t"
               "s_mov_b32 m0, s7\n\t"
               "s_nop 0\n\t"
               "buffer_load_dwordx4 %[v0], s[8:11], s7 offen nt lds\n\t"
               "buffer_load_dwordx4 %[v1], s[8:11], s7 offen offset:1024 nt lds\n\t"
               "buffer_load_dwordx4 %[v2], s[8:11], s7 offen offset:2048 nt lds\n\t"
               "buffer_load_dwordx4 %[v3], s[8:11], s7 offen offset:3072 nt lds\n\t"
               "s_add_u32 s7, s7, 0x1000\n\t"
               "s_cmp_lt_u32 s7, 0xc000\n\t"
               "s_cbranch_scc1 .Lc%=\n\t"
               "s_branch .Lskip%=\n"
               ".Lrest%=:\n\t"
               "s_getpc_b64 s[8:9]\n\t"
               "s_lshr_b32 s6, %[bid], 3\n\t"
               "v_subrev_u32 %[v0], 448, %[tid]\n\t"
               "s_and_b32 s8, s8, 0xffffff80\n\t"
               "s_and_b32 s9, s9, 0xffff\n\t"
               "s_movk_i32 s10, %[cb]\n\t"
               "s_mulk_i32 s6, 768\n\t"
               "v_cmp_gt_u32 vcc, 6, %[v0]\n\t"
               "s_mov_b32 m0, %[scr]\n\t"
               "v_lshlrev_b32 %[v0], 7, %[v0]\n\t"
               "v_and_b32 %[v1], 63, %[tid]\n\t"
               "v_mov_b32 %[v2], 0x10000\n\t"
               "v_cndmask_b32 %[v0], %[v2], %[v0], vcc\n\t"
               "buffer_load_dword %[v0], s[8:11], s6 offen lds\n\t"
               "v_cmp_eq_u32 vcc, 14, %[v1]\n\t"
               "v_lshlrev_b32 %[v3], 2, %[v1]\n\t"
               "v_add_u32 %[v3], %[ctl], %[v3]\n\t"
               "v_cndmask_b32 %[v2], 0, 8, vcc\n\t"
               "v_cmp_gt_u32 vcc, 4, %[v1]\n\t"
               "s_nop 1\n\t"
               "v_cndmask_b32 %[v2], %[v2], %[v1], vcc\n\t"
               "ds_write_b32 %[v3], %[v2]\n\t"
               "s_waitcnt lgkmcnt(0)\n\t"
               "s_barrier\n"
               ".Lskip%=:"
               : [v0] "=&v"(vo[0]), [v1] "=&v"(vo[1]), [v2] "=&v"(vo[2]), [v3] "=&v"(vo[3])
               : [ka] "s"(__builtin_amdgcn_kernarg_segment_ptr()), [tid] "v"(threadIdx.x), [bid] "s"(blockIdx.x),
                 [cb] "n"(CODEB), [scr] "n"(L_FB), [ctl] "n"(L_CTL), [fw] "n"(F - 1), [e2] "n"(0x9000)
               : "memory", "scc", "vcc", "s5", "s6", "s7", "s8", "s9", "s10", "s11");
  SCHED();
  LAS char* lds = (LAS char*)lds_raw;
  volatile LAS u32* ctl = (volatile LAS u32*)(lds + L_CTL);
  const int lane = threadIdx.x & 63;
  const int wave = __builtin_amdgcn_readfirstlane(threadIdx.x >> 6);
  const int pair = blockIdx.x >> 3, j = blockIdx.x & 7;
  const int b = pair >> 3, kvh = pair & 7;
  const int pc = (kvh + 3 * j) & 7;
  const int sidx = j * 8 + pc;
  const int didx = kvh * 8 + j;
  const __amdgpu_buffer_rsrc_t wsr = __builtin_amdgcn_make_buffer_rsrc(ws, 0, WS_END, 0x00020000);
  const u32 yin = ((u32)(b * NDEST + didx) * 512 + wave * 64 + lane) * 16;

#define DMA_GROUP(LO, R, SO, WAIT) asm volatile("s_mov_b32 m0, %0\n\ts_nop 0\n\t" \
                 "buffer_load_dwordx4 %1, %5, %6 offen nt lds\n\t" \
                 "s_waitcnt vmcnt(%7)\n\t" \
                 "buffer_load_dwordx4 %2, %5, %6 offen offset:1024 nt lds\n\t" \
                 "s_waitcnt vmcnt(%7)\n\t" \
                 "buffer_load_dwordx4 %3, %5, %6 offen offset:2048 nt lds\n\t" \
                 "s_waitcnt vmcnt(%7)\n\t" \
                 "buffer_load_dwordx4 %4, %5, %6 offen offset:3072 nt lds\n\t" \
                 "s_waitcnt vmcnt(%7)" \
                 :: "s"(LO), "v"(vo[0]), "v"(vo[1]), "v"(vo[2]), "v"(vo[3]), "s"(R), "s"(SO), "n"(WAIT) : "memory")
  const u32 tz = (u32)__builtin_amdgcn_s_memrealtime();
  const u64 kb = (u64)kc + (u64)pair * SK * (DH * 2);
  i32x4 kr;
  kr[0] = (int)(u32)kb; kr[1] = (int)(u32)(kb >> 32); kr[2] = 8 * UNITS * SLOT; kr[3] = 0x00020000;
  const u32 lbase = (u32)(size_t)lds;
  if (wave == 0) {
    __builtin_amdgcn_s_setprio(3);
    const u64 vb = (u64)vc + (u64)pair * SK * (DH * 2);
    i32x4 vr;
    vr[0] = (int)(u32)vb; vr[1] = (int)(u32)(vb >> 32); vr[2] = 8 * UNITS * SLOT; vr[3] = 0x00020000;
    u32 pub = 0, freed = 0, p0 = 0;
    int pos = 3, s, total = UNITS + SV + 1;
#pragma unroll 1
    for (s = 3; s < total; ++s) {
      if (s == UNITS + SV) {
        const u32 pool = lds_wait<0>(&ctl[C_POOL], 1u);
        p0 = ((pool >> 8) & 0xff) - (UNITS + SV);
        total = UNITS + SV + (int)(pool & 0xff);
        if (s == total) break;
      }
      const u32 pp = p0 + s;
      const u32 so = (s < UNITS + SV ? (u32)(j * UNITS + (s & (UNITS - 1))) : (pp >> 3) * UNITS + SV + (pp & 7)) * SLOT;
      if (s >= NS && freed < (u32)(s - NS + 1)) {
        freed = free_min(ctl);
        if (freed < (u32)(s - NS + 1)) {
          asm volatile("s_waitcnt vmcnt(0)" ::: "memory");
          pub = s; ctl[C_FULL] = pub;
#pragma unroll 1
          for (u32 sp = 0; sp < spin_cap(1); ++sp) { freed = free_min(ctl); if (freed >= (u32)(s - NS + 1)) break; __builtin_amdgcn_s_sleep(1); }
          MEMFENCE();
        }
      }
      i32x4 r = (s >= UNITS) ? vr : kr;
      const u32 lo = lbase + pos * SLOT;
#pragma unroll
      for (int q = 0; q < 4; ++q) {
        DMA_GROUP(lo + q * 4096, r, so + q * 4096, F - 1);
        const int landed = (16 * s + 4 * q + 5 - F) >> 4;
        if (landed > (int)pub) { pub = landed; ctl[C_FULL] = pub; }
      }
      pos = (pos + 1 == NS) ? 0 : pos + 1;
    }
    ctl[C_LAST] = 1u;
    static_assert(F <= 32 && F > 16, "tail drain");
    asm volatile("s_waitcnt vmcnt(16)" ::: "memory");
    if ((int)pub < s - 1) ctl[C_FULL] = s - 1;
    asm volatile("s_waitcnt vmcnt(0)" ::: "memory");
    ctl[C_FULL] = (u32)s | FINAL;
    __builtin_amdgcn_s_setprio(0);
  } else {
    const int n = lane & 15, g = lane >> 4;
    const u32 ka0 = n * 512 + ((g ^ ((n & 7) << 1)) << 4);
    bf16x8 kf[2][16];
    auto grab = [&](int t) {
      const int u = wave - 2 + 4 * t;
      lds_wait<2>(&ctl[C_FULL], u + 1);
      LAS char* kbase = lds + u * SLOT;
#pragma unroll
      for (int ks = 0; ks < 8; ++ks) {
        const u32 a = (ka0 ^ ((ks & 3) << 6)) + (ks >> 2) * 256;
        kf[t][2 * ks] = *(LAS bf16x8*)(kbase + a);
        kf[t][2 * ks + 1] = *(LAS bf16x8*)(kbase + a + 8192);
      }
      MEMFENCE();
      ctl[C_FREE + wave - 2] = t == 0 ? u + 4 : UNITS;
    };
    u32x2 epoch;
    {
      const int r = lane >> 3, c = lane & 7;
      const int row0 = 64 * (wave - 1) + 8 * r;
      const __amdgpu_buffer_rsrc_t rq = __builtin_amdgcn_make_buffer_rsrc((void*)wq, 0, DM * NQ * 2, 0x00020000);
      const u32 qofs = (u32)(row0 * NQ + (j * 512 + pc * 64) + c * 8) * 2;
      u32x4 w[8];
      u32 tch;
      {
        gate_at(tz, T_TOUCH);
        const int k = (pair + 4 * j) & 31;
        const u16* pt = k == 0 ? x : k == 1 ? (const u16*)(ws + 2048) : k == 2 ? wq : k == 3 ? wq + DM * NQ - 64 : k == 4 ? wo : k == 5 ? wo + DM * NQ - 64
                      : k == 6 ? w1 : k == 7 ? w2 : k == 8 ? gw : k == 9 ? (const u16*)out : (const u16*)(ws + WS_END);
        const __amdgpu_buffer_rsrc_t rt = __builtin_amdgcn_make_buffer_rsrc((void*)pt, 0, k <= 10 ? 4 : 0, 0x00020000);
        tch = __builtin_amdgcn_raw_buffer_load_b32(rt, threadIdx.x == 64 ? 0 : OOB, 0, 0);
      }
      gate_at(tz, T_WQ);
      const u32x4 xv = *(const u32x4*)(x + b * DM + row0);
      const u32 eoff = threadIdx.x == 64 ? WS_EPOCH + b * 256 : OOB;
      epoch = __builtin_amdgcn_raw_buffer_load_b64(wsr, eoff, 0, SC1);
#pragma unroll
      for (int i = 0; i < 4; ++i) {
        if (i > 0) gate_at(tz, T_WQ + 30 + i * D_WQ);
        w[2 * i] = __builtin_amdgcn_raw_buffer_load_b128(rq, qofs, i * (4 * NQ), 0);
        w[2 * i + 1] = __builtin_amdgcn_raw_buffer_load_b128(rq, qofs, i * (4 * NQ) + 2 * NQ, 0);
      }
      SCHED();
      if (wave >= 2 && wave <= 5) grab(0);
      SCHED();
      float a[8];
#pragma unroll
      for (int e = 0; e < 8; ++e) a[e] = 0.f;
#pragma unroll
      for (int i = 0; i < 4; ++i)
#pragma unroll
        for (int d = 0; d < 4; ++d) {
          a[2 * d] = dot2(pair_lo(w[2 * i][d], w[2 * i + 1][d]), xv[i], a[2 * d]);
          a[2 * d + 1] = dot2(pair_hi(w[2 * i][d], w[2 * i + 1][d]), xv[i], a[2 * d + 1]);
        }
      asm volatile("" :: "v"(tch));
      ((LAS float*)(lds + L_QRED))[(wave - 1) * 64 + 8 * c + r] = reduce8(a, lane);
      LGKM0();
      if (lane == 0) __hip_atomic_fetch_add((LAS u32*)&ctl[C_QCNT], 1u, __ATOMIC_RELAXED, __HIP_MEMORY_SCOPE_WORKGROUP);
    }

    if (wave == 1) {
      __builtin_amdgcn_s_setprio(2);
      u64 z = ((u64)epoch[0] | ((u64)epoch[1] << 32)) + 0x9E3779B97F4A7C15ull;
      const u64 epoch_next = z;
      z = (z ^ (z >> 30)) * 0xBF58476D1CE4E5B9ull; z = (z ^ (z >> 27)) * 0x94D049BB133111EBull; z ^= z >> 31;
      const u32 tagA = __builtin_amdgcn_readfirstlane((u32)z);
      u32 tagB = __builtin_amdgcn_readfirstlane((u32)(z >> 32));
      if (tagB == tagA) tagB = ~tagA;
      if (lane == 0) {
        ctl[C_TAGA] = tagA; ctl[C_TAGB] = tagB;
        ctl[C_EP0] = (u32)epoch_next; ctl[C_EP1] = (u32)(epoch_next >> 32);
      }
      lds_wait<0>(&ctl[C_QCNT], 7u);
      {
        float s = 0.f;
#pragma unroll
        for (int w = 0; w < 7; ++w) s += ((LAS float*)(lds + L_QRED))[w * 64 + lane];
        const u32 d = pk_bf16(s, dpp<QP_X1>(s));
        const u32 d2 = dppu<QP_X2>(d);
        put_granule(wsr, (lane & 3) == 0 ? WS_Q + (((b * 8 + j) * 128 + pc * 16) + (lane >> 2)) * 16 : OOB, d, d2, tagA, tagB);
        const u32 off[2] = {WS_Q + (pair * 128 + lane) * 16u, WS_Q + (pair * 128 + 64 + lane) * 16u};
        u32 d0[2], d1[2];
        __builtin_amdgcn_s_sleep(40);
        gather<2, 8>(wsr, off, tagA, tagB, d0, d1);
#pragma unroll
        for (int i = 0; i < 2; ++i) *(LAS u32x2*)(lds + L_QS + (i * 64 + lane) * 8) = u32x2{d0[i], d1[i]};
        LGKM0();
      }
      ctl[C_QRDY] = 1u;
      bf16x8 qf[8];
#pragma unroll
      for (int ks = 0; ks < 8; ++ks) qf[ks] = *(LAS bf16x8*)(lds + L_QS + (n & 1) * 512 + ks * 64 + g * 16);
      int pos = (4 * KREG) % NS;
      u32 full = 0;
#pragma unroll 1
      for (int u = 4 * KREG; u < UNITS; ++u) {
        if (full < (u32)(u + 1)) full = lds_wait<1>(&ctl[C_FULL], u + 1);
        MEMFENCE();
        LAS char* kbase = lds + pos * SLOT;
        pos = (pos + 1 == NS) ? 0 : pos + 1;
        bf16x8 k0[8], k1[8];
#pragma unroll
        for (int ks = 0; ks < 8; ++ks) {
          const u32 a = (ka0 ^ ((ks & 3) << 6)) + (ks >> 2) * 256;
          k0[ks] = *(LAS bf16x8*)(kbase + a);
          k1[ks] = *(LAS bf16x8*)(kbase + a + 8192);
        }
        MEMFENCE();
        ctl[C_FREE1] = u + 1 < UNITS ? u + 1 : 0xffffu;
        SCHED();
        f32x4 s0 = {0.f, 0.f, 0.f, 0.f}, s1 = {0.f, 0.f, 0.f, 0.f};
#pragma unroll
        for (int ks = 0; ks < 8; ++ks) {
          s0 = __builtin_amdgcn_mfma_f32_16x16x32_bf16(k0[ks], qf[ks], s0, 0, 0, 0);
          s1 = __builtin_amdgcn_mfma_f32_16x16x32_bf16(k1[ks], qf[ks], s1, 0, 0, 0);
        }
        const u32x4 sw = {pk_bf16(s0[0], s0[1]), pk_bf16(s0[2], s0[3]), pk_bf16(s1[0], s1[1]), pk_bf16(s1[2], s1[3])};
        if (n < 2) *(LAS u32x4*)(lds + L_SB + u * 128 + g * 32 + n * 16) = sw;
      }
      lds_wait<1>(&ctl[C_SCNT], 4u);
      constexpr float C = 0.0625f * 1.4426950408889634f;
      LGKM0();
      u32x4 sv[4];
#pragma unroll
      for (int i = 0; i < 4; ++i) sv[i] = *(LAS u32x4*)(lds + L_SB + (i * 64 + lane) * 16);
      __builtin_amdgcn_raw_buffer_store_b128(sv[3], wsr, WS_S + (pair * 8 + j) * 1024 + lane * 16, 0, SC1);
      float mx = -INFINITY;
#pragma unroll
      for (int i = 0; i < 4; ++i)
#pragma unroll
        for (int e = 0; e < 4; ++e) mx = fmaxf(mx, fmaxf(bf_lo(sv[i][e]), bf_hi(sv[i][e])));
      mx = fmaxf(mx, dpp<QP_X2>(mx)); mx = fmaxf(mx, dpp<ROR4>(mx)); mx = fmaxf(mx, dpp<ROR8>(mx));
      const float mc = xrow_max(mx) * C;
      float ls = 0.f;
#pragma unroll
      for (int i = 0; i < 4; ++i) {
#pragma unroll
        for (int e = 0; e < 4; ++e) {
          const float p0 = __builtin_amdgcn_exp2f(fmaf(bf_lo(sv[i][e]), C, -mc)), p1 = __builtin_amdgcn_exp2f(fmaf(bf_hi(sv[i][e]), C, -mc));
          ls += p0 + p1;
          sv[i][e] = pk_bf16(p0, p1);
        }
        if (i < 3) *(LAS u32x4*)(lds + L_SB + (i * 64 + lane) * 16) = sv[i];
      }
      LGKM0();
      ctl[C_PRDY] = 1u;
      ls += dpp<QP_X2>(ls); ls += dpp<ROR4>(ls); ls += dpp<ROR8>(ls);
      ls = swap16_add(ls, ls); ls = swap32_add(ls, ls);
      u32 ca = lds_ld(&ctl[C_FULL]) + 1;
      ca += (ca ^ CKB) & 1;
      ca = lds_wait<1>(&ctl[C_FULL], min(ca, (u32)(UNITS + SV - 3)));
      const u32 ta = (u32)__builtin_amdgcn_s_memrealtime();
      lds_wait<8>(&ctl[C_FULL], UNITS + CKB - 1);
      const u32 cb = lds_wait<1>(&ctl[C_FULL], min(max(ca + 2, (u32)(UNITS + CKB)), (u32)(UNITS + SV - 3)));
      const u32 tb = (u32)__builtin_amdgcn_s_memrealtime();
      const float pace = (float)(tb - ta) / (float)(cb - ca);
      const u32 arrive = tb + (u32)(int)(pace * (float)(int)(UNITS + CKB - cb));
#pragma unroll 1
      for (u32 spn = 0; spn < spin_cap(2) && free_min(ctl) < (u32)(UNITS + CKV + 1); ++spn) __builtin_amdgcn_s_sleep(2);
      MEMFENCE();
      const u32 vmax = lds_ld(&ctl[C_VMAX]);
      asm volatile("s_waitcnt vmcnt(0)" ::: "memory");
      put_granule(wsr, lane < 4 ? WS_ML + ((pair * 8 + j) * 4 + lane) * 16 : OOB, lane < 2 ? __float_as_uint(mc) : lane == 2 ? arrive : vmax,
                  __float_as_uint(lane < 2 ? ls : pace), tagA, tagB);
      lds_wait<8>(&ctl[C_FULL], UNITS + CKG);
      u32 d0[1], d1[1];
      {
        const u32 off[1] = {WS_ML + (pair * 32 + (lane & 31)) * 16u};
        gather<1, 8>(wsr, off, tagA, tagB, d0, d1);
      }
      u32 first, count;
      {
        float av[8], rv[8], sr = 0.f, sa = 0.f;
        const u32 a0 = __builtin_amdgcn_readlane(d0[0], 2);
        float mean = 0.f;
#pragma unroll
        for (int i = 0; i < 8; ++i) { rv[i] = __uint_as_float(__builtin_amdgcn_readlane(d1[0], 4 * i + 2)); mean += 0.125f * rv[i]; }
#pragma unroll
        for (int i = 0; i < 8; ++i) {
          const float pcs = mean + SHR * (rv[i] - mean);
          av[i] = (float)(int)(__builtin_amdgcn_readlane(d0[0], 4 * i + 2) - a0) + pcs * (float)(SV - CKB);
          rv[i] = 1.f / pcs;
          sr += rv[i]; sa += av[i] * rv[i];
        }
        const float E = ((float)NPOOL + sa) / sr;
        float tot = 0.f;
#pragma unroll
        for (int i = 0; i < 8; ++i) { av[i] = fmaxf((E - av[i]) * rv[i], 0.f); tot += av[i]; }
        const float scale = (float)NPOOL / tot;
        int lo = 0, hi = 0;
        bool ok = true;
        {
          int nf[8], left = NPOOL;
          float fr[8];
#pragma unroll
          for (int i = 0; i < 8; ++i) { const float xs = av[i] * scale; nf[i] = (int)xs; fr[i] = xs - (float)nf[i]; left -= nf[i]; }
          int prev = 0;
#pragma unroll
          for (int i = 0; i < 8; ++i) {
            int rank = 0;
#pragma unroll
            for (int k = 0; k < 8; ++k) rank += (k != i) && (fr[k] > fr[i] || (fr[k] == fr[i] && k < i));
            const int c = nf[i] + (rank < left);
            ok = ok && c >= 0 && c <= NMAX;
            if (i == j) { lo = prev; hi = prev + c; }
            prev += c;
          }
          ok = ok && prev == NPOOL;
        }
        first = __builtin_amdgcn_readfirstlane(ok ? lo : 8 * j);
        count = __builtin_amdgcn_readfirstlane(ok ? hi - lo : 8);
      }
      ctl[C_POOL] = 0x10000u | (first << 8) | count;
      u32x4 sp[3];
#pragma unroll
      for (int i = 0; i < 3; ++i)
        sp[i] = __builtin_amdgcn_raw_buffer_load_b128(wsr, (u32)(i * 64 + lane) < count * 8 ? WS_S + pair * 8192 + first * 128 + (i * 64 + lane) * 16 : OOB, 0, SC1);
      const float ms = (lane & 2) ? -INFINITY : __uint_as_float(d0[0]);
      float M = fmaxf(ms, dpp<QP_X2>(ms)); M = fmaxf(M, dpp<ROR4>(M)); M = fmaxf(M, dpp<ROR8>(M)); M = xrow_max(M);
      float L = (lane & 2) ? 0.f : __builtin_amdgcn_exp2f(ms - M) * __uint_as_float(d1[0]);
      L += dpp<QP_X2>(L); L += dpp<ROR4>(L); L += dpp<ROR8>(L); L = swap16_add(L, L);
      const float il = 1.f / L;
      const float sc = __builtin_amdgcn_exp2f(mc - M) * il;
      if (lane < 2) { ctl[C_SC0 + lane] = __float_as_uint(sc); ctl[C_IL0 + lane] = __float_as_uint(il); }
      {
        u32 vm = 0;
#pragma unroll
        for (int i = 0; i < 8; ++i) vm = max(vm, (u32)__builtin_amdgcn_readlane(d0[0], 4 * i + 3));
        const u32 be = (vm >> 7) + 3;
        ctl[C_GRID] = be < 254 ? (be << 23) | 0x400000u : 0u;
      }
#pragma unroll 1
      for (u32 spn = 0; spn < spin_cap(2) && free_min(ctl) < (u32)(UNITS + NMAX - 8); ++spn) __builtin_amdgcn_s_sleep(2);
      MEMFENCE();
#pragma unroll
      for (int i = 0; i < 3; ++i) {
#pragma unroll
        for (int e = 0; e < 4; ++e)
          sp[i][e] = pk_bf16(__builtin_amdgcn_exp2f(fmaf(bf_lo(sp[i][e]), C, -M)), __builtin_amdgcn_exp2f(fmaf(bf_hi(sp[i][e]), C, -M)));
        if ((u32)(i * 64 + lane) < count * 8) *(LAS u32x4*)(lds + L_SB + ((SV * 8 + i * 64 + lane) & 255) * 16) = sp[i];
      }
      LGKM0();
      ctl[C_CRDY] = 1u;
      __builtin_amdgcn_s_setprio(0);
    } else {
      if (wave <= 5) {
        const int cw = wave - 2;
        grab(1);
        lds_wait<4>(&ctl[C_QRDY], 1u);
        f32x4 sa[KREG][2];
#pragma unroll
        for (int t = 0; t < KREG; ++t) { sa[t][0] = f32x4{0.f, 0.f, 0.f, 0.f}; sa[t][1] = f32x4{0.f, 0.f, 0.f, 0.f}; }
#pragma unroll
        for (int ks = 0; ks < 8; ++ks) {
          const bf16x8 qv = *(LAS bf16x8*)(lds + L_QS + (n & 1) * 512 + ks * 64 + g * 16);
#pragma unroll
          for (int t = 0; t < KREG; ++t) {
            sa[t][0] = __builtin_amdgcn_mfma_f32_16x16x32_bf16(kf[t][2 * ks], qv, sa[t][0], 0, 0, 0);
            sa[t][1] = __builtin_amdgcn_mfma_f32_16x16x32_bf16(kf[t][2 * ks + 1], qv, sa[t][1], 0, 0, 0);
          }
        }
#pragma unroll
        for (int t = 0; t < KREG; ++t) {
          const u32x4 sw = {pk_bf16(sa[t][0][0], sa[t][0][1]), pk_bf16(sa[t][0][2], sa[t][0][3]), pk_bf16(sa[t][1][0], sa[t][1][1]), pk_bf16(sa[t][1][2], sa[t][1][3])};
          if (n < 2) *(LAS u32x4*)(lds + L_SB + (cw + 4 * t) * 128 + g * 32 + n * 16) = sw;
        }
        LGKM0();
        if (lane == 0) __hip_atomic_fetch_add((LAS u32*)&ctl[C_SCNT], 1u, __ATOMIC_RELAXED, __HIP_MEMORY_SCOPE_WORKGROUP);
      }
      lds_wait<8>(&ctl[C_QRDY], 1u);
      const u32 tagA = lds_ld(&ctl[C_TAGA]), tagB = lds_ld(&ctl[C_TAGB]);

      if (wave <= 5) {
        const int cw = wave - 2;
        const int c8 = 16 * cw + n;
        const bool dlive = c8 < 2 * NDEST;
        const u32 gofs = dlive ? ((u32)(b * NDEST + (c8 >> 1)) * 512 + sidx * 8 + (c8 & 1) * 4 + (g >> 1) + 2 * (g & 1)) * 16 : OOB;
        const u32 wofs = dlive ? (u32)(4 * g * DM + c8 * 8) * 2 : OOB;
        const int vq = n >> 2, vp = n & 3, vrow = 4 * g + vq;
        const u32 va0 = ((vrow * 512 + (((vp >> 1) ^ ((vrow & 7) << 1)) << 4) + (vp & 1) * 8) ^ ((cw & 1) << 7)) + (cw >> 1) * 256;
        f32x4 acc[4];
        float st0 = 0.f, st1 = 0.f, il = 0.f, big = 0.f;
        u16x2 vm = {0, 0};
#pragma unroll
        for (int t = 0; t < 4; ++t) acc[t] = f32x4{0.f, 0.f, 0.f, 0.f};
        int pos = UNITS % NS;
        u32 full = 0;
        auto pick = [&](int e) {
          const float a0 = (g & 1) ? acc[e][1] : acc[e][0], a2 = (g & 1) ? acc[2 + e][1] : acc[2 + e][0];
          return (g & 2) ? a2 : a0;
        };
        auto pv_step = [&](int u) {
          const bf16x8 pf = *(LAS bf16x8*)(lds + L_SB + (u & 31) * 128 + g * 32 + (n & 1) * 16);
          MEMFENCE();
          if (full < (u32)(UNITS + u + 1)) full = lds_wait<0>(&ctl[C_FULL], UNITS + u + 1);
          MEMFENCE();
          LAS char* vbase = lds + pos * SLOT;
          pos = (pos + 1 == NS) ? 0 : pos + 1;
          s16x4 lo[4], hi[4];
#pragma unroll
          for (int t = 0; t < 4; ++t) {
            const u32 a = va0 ^ (t << 5);
            lo[t] = __builtin_amdgcn_ds_read_tr16_b64_v4i16((LAS s16x4*)(vbase + a));
            hi[t] = __builtin_amdgcn_ds_read_tr16_b64_v4i16((LAS s16x4*)(vbase + a + 8192));
          }
          MEMFENCE();
          ctl[C_FREE + cw] = UNITS + u + 1;
          SCHED();
#pragma unroll
          for (int t = 0; t < 4; ++t) {
            const s16x8 vv = __builtin_shufflevector(lo[t], hi[t], 0, 1, 2, 3, 4, 5, 6, 7);
            acc[t] = __builtin_amdgcn_mfma_f32_16x16x32_bf16(pf, __builtin_bit_cast(bf16x8, vv), acc[t], 0, 0, 0);
          }
          if (u < CKV) {
#pragma unroll
            for (int t = 0; t < 4; ++t) {
              const u32x2 a = __builtin_bit_cast(u32x2, lo[t]), c = __builtin_bit_cast(u32x2, hi[t]);
              vm = __builtin_elementwise_max(vm, __builtin_bit_cast(u16x2, a[0] & 0x7fff7fffu)); vm = __builtin_elementwise_max(vm, __builtin_bit_cast(u16x2, a[1] & 0x7fff7fffu));
              vm = __builtin_elementwise_max(vm, __builtin_bit_cast(u16x2, c[0] & 0x7fff7fffu)); vm = __builtin_elementwise_max(vm, __builtin_bit_cast(u16x2, c[1] & 0x7fff7fffu));
            }
            if (u == CKV - 1) __hip_atomic_fetch_max((LAS u32*)&ctl[C_VMAX], (u32)max(vm[0], vm[1]), __ATOMIC_RELAXED, __HIP_MEMORY_SCOPE_WORKGROUP);
          } else if (u >= SV) {
            st0 += fmaf(pick(0), il, big) - big; st1 += fmaf(pick(1), il, big) - big;
#pragma unroll
            for (int t = 0; t < 4; ++t) acc[t] = f32x4{0.f, 0.f, 0.f, 0.f};
          }
        };
        const u32 pout = WS_P + ((((pair * 8 + (g & 1) * 4 + cw) * 8 + j) * 32) + (g >> 1) * 16 + n) * 16;
        lds_wait<8>(&ctl[C_GRDY], 1u);
        const float gate = __uint_as_float(lds_ld(&ctl[C_GATE]));
        lds_wait<8>(&ctl[C_PRDY], 1u);
        __builtin_amdgcn_s_setprio(1);
        int uend = SV + 1;
#pragma unroll 1
        for (int u = 0; u < uend; ++u) {
          if (u == SV) {
            lds_wait<1>(&ctl[C_CRDY], 1u);
            const float sc = __uint_as_float(ctl[C_SC0 + (g & 1)]);
            il = __uint_as_float(ctl[C_IL0 + (g & 1)]);
            uend = SV + (int)(lds_ld(&ctl[C_POOL]) & 0xff);
            big = __uint_as_float(ctl[C_GRID]);
            st0 = fmaf(pick(0), sc, big) - big; st1 = fmaf(pick(1), sc, big) - big;
#pragma unroll
            for (int t = 0; t < 4; ++t) acc[t] = f32x4{0.f, 0.f, 0.f, 0.f};
            if (u >= uend) break;
          }
          pv_step(u);
        }
        put_granule(wsr, pout, __float_as_uint(st0), __float_as_uint(st1), tagA, tagB);
        u32x4 ua[2][4], ub[2][4];
        {
          const __amdgpu_buffer_rsrc_t ro = __builtin_amdgcn_make_buffer_rsrc((void*)(wo + (size_t)(kvh * 512 + j * 64) * DM), 0, 64 * DM * 2, 0x00020000);
          SCHED();
#pragma unroll
          for (int ks = 0; ks < 2; ++ks)
#pragma unroll
            for (int d = 0; d < 4; ++d) {
              ua[ks][d] = __builtin_amdgcn_raw_buffer_load_b128(ro, wofs, (32 * ks + d) * (DM * 2), 0);
              ub[ks][d] = __builtin_amdgcn_raw_buffer_load_b128(ro, wofs, (32 * ks + d + 16) * (DM * 2), 0);
            }
          SCHED();
        }
        float y[8];
        u32 wr[2][4][8];
#pragma unroll
        for (int ks = 0; ks < 2; ++ks)
#pragma unroll
          for (int d = 0; d < 4; ++d)
#pragma unroll
            for (int e2 = 0; e2 < 4; ++e2) {
              wr[ks][d][2 * e2] = pair_lo(ua[ks][d][e2], ub[ks][d][e2]);
              wr[ks][d][2 * e2 + 1] = pair_hi(ua[ks][d][e2], ub[ks][d][e2]);
              asm volatile("" : "+v"(wr[ks][d][2 * e2]), "+v"(wr[ks][d][2 * e2 + 1]));
            }
        lds_wait<0>(&ctl[C_OCNT], 4u);
        const f32x2 fb = *(LAS f32x2*)(lds + L_FB + cw * 512 + lane * 8);
        u32x4 ov0, ov1;
        {
          f32x4 pa[2][4];
#pragma unroll
          for (int c = 0; c < 2; ++c)
#pragma unroll
            for (int i = 0; i < 4; ++i) pa[c][i] = *(LAS f32x4*)(lds + L_PA + c * 256 + (i >> 1) * 128 + g * 32 + (i & 1) * 16);
#pragma unroll
          for (int i = 0; i < 4; ++i) pa[0][i] += pa[1][i];
          ov0 = u32x4{pk_bf16(pa[0][0][0], pa[0][0][1]), pk_bf16(pa[0][0][2], pa[0][0][3]), pk_bf16(pa[0][1][0], pa[0][1][1]), pk_bf16(pa[0][1][2], pa[0][1][3])};
          ov1 = u32x4{pk_bf16(pa[0][2][0], pa[0][2][1]), pk_bf16(pa[0][2][2], pa[0][2][3]), pk_bf16(pa[0][3][0], pa[0][3][1]), pk_bf16(pa[0][3][2], pa[0][3][3])};
        }
#pragma unroll
        for (int e = 0; e < 8; ++e) y[e] = 0.f;
#pragma unroll
        for (int d = 0; d < 4; ++d)
#pragma unroll
          for (int e = 0; e < 8; ++e) y[e] = dot2(wr[0][d][e], ov0[d], y[e]);
#pragma unroll
        for (int d = 0; d < 4; ++d)
#pragma unroll
          for (int e = 0; e < 8; ++e) y[e] = dot2(wr[1][d][e], ov1[d], y[e]);
        float y4[4];
#pragma unroll
        for (int e = 0; e < 4; ++e) y4[e] = swap32_add(y[(e & 1) + 4 * (e >> 1)], y[(e & 1) + 4 * (e >> 1) + 2]);
        put_granule(wsr, WS_Y + gofs, __float_as_uint(fmaf(gate, swap16_add(y4[0], y4[2]), fb[0])), __float_as_uint(fmaf(gate, swap16_add(y4[1], y4[3]), fb[1])), tagA, tagB);
      } else if (wave == 7) {
        if (sidx < NFFN) {
          const int r = lane >> 2, c = lane & 3;
          const __amdgpu_buffer_rsrc_t r1 = __builtin_amdgcn_make_buffer_rsrc((void*)w1, 0, DM * DFF * 2, 0x00020000);
          const u32 mofs = (u32)(28 * r * DFF + sidx * 32 + c * 8) * 2;
          if (lane < 56) *(LAS u32x4*)(lds + L_QRED + lane * 16) = *(const u32x4*)(x + b * DM + lane * 8);
          u32x4 w[28];
          u32 xv[14];
#pragma unroll
          for (int i = 0; i < 14; ++i) {
            if ((i & 1) == 0) gate_at(tz, T_W1 + i * D_W1);
            int ip = i + 2 * (pc & 1); ip = ip >= 14 ? ip - 14 : ip;
            w[2 * i] = __builtin_amdgcn_raw_buffer_load_b128(r1, mofs, ip * (4 * DFF), 0);
            w[2 * i + 1] = __builtin_amdgcn_raw_buffer_load_b128(r1, mofs, ip * (4 * DFF) + 2 * DFF, 0);
            xv[i] = *(LAS u32*)(lds + L_QRED + r * 56 + ip * 4);
          }
          float a[8];
#pragma unroll
          for (int e = 0; e < 8; ++e) a[e] = 0.f;
#pragma unroll
          for (int i = 0; i < 14; ++i)
#pragma unroll
            for (int d = 0; d < 4; ++d) {
              a[2 * d] = dot2(pair_lo(w[2 * i][d], w[2 * i + 1][d]), xv[i], a[2 * d]);
              a[2 * d + 1] = dot2(pair_hi(w[2 * i][d], w[2 * i + 1][d]), xv[i], a[2 * d + 1]);
            }
          float dsum = reduce8(a, lane);
          dsum += __shfl_xor(dsum, 4);
          const float hv = fmaxf(rbf(dsum), 0.f);
          const int unit = 8 * c + (lane >> 3);
          ((LAS u16*)(lds + L_HS))[2 * (unit & 15) + (unit >> 4)] = (u16)(__float_as_uint(hv) >> 16);
          LGKM0();
          ctl[C_HRDY] = 1u;
        }
      } else {
        {
          const u32x4 xv = *(const u32x4*)(x + b * DM + (lane < 56 ? lane : 0) * 8), gv = *(const u32x4*)(gw + (lane < 56 ? lane : 0) * 8);
          float gsum = 0.f;
#pragma unroll
          for (int i = 0; i < 4; ++i) gsum += rbf(bf_lo(xv[i]) * bf_lo(gv[i])) + rbf(bf_hi(xv[i]) * bf_hi(gv[i]));
          gsum = lane < 56 ? gsum : 0.f;
#pragma unroll
          for (int o = 1; o < 64; o <<= 1) gsum += __shfl_xor(gsum, o);
          ctl[C_GATE] = __float_as_uint(rbf(1.f / (1.f + __expf(-rbf(gsum)))));
          LGKM0();
          ctl[C_GRDY] = 1u;
        }
        {
          lds_wait<16>(&ctl[C_FULL], 18u + ((pair + j) & 7));
          const __amdgpu_buffer_rsrc_t rv = __builtin_amdgcn_make_buffer_rsrc((void*)(vc + (size_t)pair * SK * DH), 0, SK * DH * 2, 0x00020000);
          const u32 t = __builtin_amdgcn_raw_buffer_load_b32(rv, lane < 2 ? (u32)((j ^ (lane * 4)) * (UNITS * SLOT) + lane * 4096) : OOB, 0, 2);
          asm volatile("" :: "v"(t));
        }
      }
    }
  }

  if (wave == 1 || wave == 6) {
    const int n = lane & 15, g = lane >> 4, fw = wave == 1 ? 2 : 0;
    const __amdgpu_buffer_rsrc_t r2 = __builtin_amdgcn_make_buffer_rsrc((void*)(w2 + (size_t)(sidx < NFFN ? sidx * 32 : 0) * DM), 0, sidx < NFFN ? 32 * DM * 2 : 0, 0x00020000);
    lds_wait<8>(&ctl[C_GRDY], 1u);
    if (sidx < NFFN) lds_wait<8>(&ctl[C_HRDY], 1u);
    const float og = rbf(1.f - __uint_as_float(lds_ld(&ctl[C_GATE])));
    u32x4 hv = *(LAS u32x4*)(lds + L_HS + g * 16);
    if (sidx >= NFFN) hv = u32x4{0u, 0u, 0u, 0u};
    const u32 wofs = (u32)(4 * g * DM + (16 * fw + n) * 8) * 2;
    const u32 wof2 = (fw == 0 || n < 8) ? wofs + 256 : OOB;
    lds_wait<16>(&ctl[C_POOL], 1u);
    lds_wait<2>(&ctl[C_LAST], 1u);
    u32x4 ua[2][4], ub[2][4];
#pragma unroll
    for (int k = 0; k < 2; ++k)
#pragma unroll
      for (int d = 0; d < 4; ++d) {
        ua[k][d] = __builtin_amdgcn_raw_buffer_load_b128(r2, k ? wof2 : wofs, d * (DM * 2), 0);
        ub[k][d] = __builtin_amdgcn_raw_buffer_load_b128(r2, k ? wof2 : wofs, (d + 16) * (DM * 2), 0);
      }
    SCHED();
#pragma unroll
    for (int k = 0; k < 2; ++k) {
      float f[8];
#pragma unroll
      for (int e = 0; e < 8; ++e) f[e] = 0.f;
#pragma unroll
      for (int d = 0; d < 4; ++d)
#pragma unroll
        for (int e2 = 0; e2 < 4; ++e2) {
          f[2 * e2] = dot2(pair_lo(ua[k][d][e2], ub[k][d][e2]), hv[d], f[2 * e2]);
          f[2 * e2 + 1] = dot2(pair_hi(ua[k][d][e2], ub[k][d][e2]), hv[d], f[2 * e2 + 1]);
        }
      float f4[4];
#pragma unroll
      for (int e = 0; e < 4; ++e) f4[e] = swap32_add(f[(e & 1) + 4 * (e >> 1)], f[(e & 1) + 4 * (e >> 1) + 2]);
      *(LAS f32x2*)(lds + L_FB + (fw + k) * 512 + lane * 8) = f32x2{swap16_add(f4[0], f4[2]) * og, swap16_add(f4[1], f4[3]) * og};
    }
    MEMFENCE();
    if (lane == 0) __hip_atomic_fetch_add((LAS u32*)&ctl[C_OCNT], 1u, __ATOMIC_RELAXED, __HIP_MEMORY_SCOPE_WORKGROUP);
  } else if (wave == 0 || wave == 7) {
    const int pw = wave & 1;
    const u32 tagA = lds_ld(&ctl[C_TAGA]), tagB = lds_ld(&ctl[C_TAGB]);
    if (wave == 7) {
      lds_wait<16>(&ctl[C_POOL], 1u);
      lds_wait<2>(&ctl[C_LAST], 1u);
      const u32 t = __builtin_amdgcn_raw_buffer_load_b32(wsr, didx < NDEST ? WS_Y + (u32)(b * NDEST + didx) * 8192 + lane * 128 : OOB, 0, SC1);
      const u32 t2 = __builtin_amdgcn_raw_buffer_load_b32(wsr, lane < 32 ? WS_P + (pair * 8 + j) * 4096 + lane * 128 : OOB, 0, SC1);
      asm volatile("" :: "v"(t), "v"(t2));
    }
    lds_wait<2>(&ctl[C_FULL], FINAL);
    __builtin_amdgcn_s_sleep(12);
    const u32 pb = WS_P + ((pair * 8 + j) * 256 + pw * 128 + lane) * 16u;
    const u32 offs[2] = {pb, pb + 1024};
    u32 d0[2], d1[2];
    gather<2, 0>(wsr, offs, tagA, tagB, d0, d1);
    const float a0 = __uint_as_float(d0[0]) + __uint_as_float(d0[1]), a1 = __uint_as_float(d1[0]) + __uint_as_float(d1[1]);
    *(LAS f32x2*)(lds + L_PA + pw * 256 + (lane & 31) * 8) = f32x2{swap32_add(a0, a0), swap32_add(a1, a1)};
    MEMFENCE();
    if (lane == 0) __hip_atomic_fetch_add((LAS u32*)&ctl[C_OCNT], 1u, __ATOMIC_RELAXED, __HIP_MEMORY_SCOPE_WORKGROUP);
  }

  if (didx >= NDEST) return;
  {
    const u32 tagA = lds_ld(&ctl[C_TAGA]), tagB = lds_ld(&ctl[C_TAGB]);
    lds_wait<1>(&ctl[C_OCNT], 4u);
    const u32 off[1] = {WS_Y + yin};
    u32 d0[1], d1[1];
    gather<1, 0>(wsr, off, tagA, tagB, d0, d1);
    const float z = inbox_sum(__uint_as_float(d0[0]), __uint_as_float(d1[0]));
    ((LAS float*)(lds + L_PY))[wave * 16 + 2 * (lane & 7) + (lane >> 5)] = z;
    MEMFENCE();
    if (lds_inc(&ctl[C_YCNT], lane) == 7u) {
      const int c = lane & 15;
      float p[8];
#pragma unroll
      for (int w = 0; w < 8; ++w) p[w] = ((LAS float*)(lds + L_PY))[w * 16 + c];
      const float res = ((p[0] + p[1]) + (p[2] + p[3])) + ((p[4] + p[5]) + (p[6] + p[7]));
      const __amdgpu_buffer_rsrc_t ro = __builtin_amdgcn_make_buffer_rsrc(out, 0, 4 * DM * 2, 0x00020000);
      __builtin_amdgcn_raw_buffer_store_b16(__builtin_bit_cast(u16, (__bf16)res), ro, lane < 16 ? (b * DM + didx * 16 + c) * 2 : OOB, 0, SC1);
      const u32x2 en = {ctl[C_EP0], ctl[C_EP1]};
      __builtin_amdgcn_raw_buffer_store_b64(en, wsr, (didx == 0 && lane == 0) ? WS_EPOCH + b * 256 : OOB, 0, SC1);
    }
  }
}

extern "C" void kernel_launch(void* const* d_in, const int* in_sizes, int n_in, void* d_out, int out_size, void* d_ws, size_t ws_size, hipStream_t stream) {
  fused_kernel<<<256, 512, 0, stream>>>((const u16*)d_in[1], (const u16*)d_in[3], (const u16*)d_in[0], (char*)d_ws, (const u16*)d_in[2],
                                        (u16*)d_out, (const u16*)d_in[4], (const u16*)d_in[7], (const u16*)d_in[6], (const u16*)d_in[8]);
}
```
